```python
import torch

def _softmax(x, axis=-1):
    return torch.softmax(x, dim=axis)
def _gelu(x):
    return torch.nn.functional.gelu(x, approximate="tanh")
def _silu(x):
    return torch.nn.functional.silu(x)
def _sigmoid(x):
    return torch.sigmoid(x)
def _layernorm(x, axis=-1, eps=1e-5):
    m = x.mean(dim=axis, keepdim=True)
    v = x.var(dim=axis, keepdim=True, unbiased=False)
    return (x - m) / torch.sqrt(v + eps)
def _scatter_add(tgt, idx, src, axis=0):
    return tgt.clone().index_add_(axis, idx, src)
def _segment_sum(src, seg, n):
    out = torch.zeros((n,) + src.shape[1:], dtype=src.dtype)
    return out.index_add_(0, seg, src)
def _pad(x, pw):
    flat = tuple(v for pair in reversed(pw) for v in pair)
    return torch.nn.functional.pad(x, flat)

kernel_name = 'gated_mix__spectral_gfnet__ssm_conv_s5__prompt'

def setup_inputs():

    _w = (lambda a: (lambda t: t.to(torch.bfloat16) if t.is_floating_point() else t)(torch.as_tensor(a)))
    def _A_setup():
        B, S, D = 1, 2048, 640
        Df = D // 2 + 1
        return {
            "x": _w((lambda *s: torch.randn(*s, dtype=torch.bfloat16) * 0.05)(B, S, D)),
            "w_r": _w((lambda *s: torch.randn(*s, dtype=torch.bfloat16) * 0.8)(S, Df) * 0.02),
            "w_i": _w((lambda *s: torch.randn(*s, dtype=torch.bfloat16) * 0.8)(S, Df) * 0.02),
        }
    _A_in = _A_setup()

    def _B_setup():
        B, S, D = 1, 2048, 640
        P = D
        Lambda_re = -0.5 * (lambda _x: torch.exp(torch.as_tensor(_x)))((lambda *s: torch.randn(*s, dtype=torch.bfloat16) * 0.2)(P))
        Lambda_im = (lambda *s: torch.randn(*s, dtype=torch.bfloat16) * 0.2)(P) * 1.0
        B_re = (lambda *s: torch.randn(*s, dtype=torch.bfloat16) * 0.2)(P, D) * (D ** -0.5)
        B_im = (lambda *s: torch.randn(*s, dtype=torch.bfloat16) * 0.2)(P, D) * (D ** -0.5)
        C_re = (lambda *s: torch.randn(*s, dtype=torch.bfloat16) * 0.2)(D, P) * (P ** -0.5)
        C_im = (lambda *s: torch.randn(*s, dtype=torch.bfloat16) * 0.2)(D, P) * (P ** -0.5)
        log_dt = (lambda *s: torch.randn(*s, dtype=torch.bfloat16) * 0.2)(P) * 0.3 - 2.0
        Dskip = (lambda *s: torch.randn(*s, dtype=torch.bfloat16) * 0.05)(D)
        return {
            "x": _w((lambda *s: torch.randn(*s, dtype=torch.bfloat16) * 0.05)(B, S, D)),
            "Lambda_re": _w(Lambda_re),
            "Lambda_im": _w(Lambda_im),
            "B_re": _w(B_re),
            "B_im": _w(B_im),
            "C_re": _w(C_re),
            "C_im": _w(C_im),
            "log_dt": _w(log_dt),
            "Dskip": _w(Dskip),
        }
    _B_in = _B_setup()

    return {
        "A_x": _A_in["x"],
        "A_w_r": _A_in["w_r"],
        "A_w_i": _A_in["w_i"],
        "B_x": _B_in["x"],
        "B_Lambda_re": _B_in["Lambda_re"],
        "B_Lambda_im": _B_in["Lambda_im"],
        "B_B_re": _B_in["B_re"],
        "B_B_im": _B_in["B_im"],
        "B_C_re": _B_in["C_re"],
        "B_C_im": _B_in["C_im"],
        "B_log_dt": _B_in["log_dt"],
        "B_Dskip": _B_in["Dskip"],
        "gate_w": _w((lambda *s: torch.randn(*s, dtype=torch.bfloat16) * 0.05)(640) * 16.0),
    }

def reference(A_x, A_w_r, A_w_i, B_x, B_Lambda_re, B_Lambda_im, B_B_re, B_B_im, B_C_re, B_C_im, B_log_dt, B_Dskip, gate_w):
    def _A_ref(x, w_r, w_i):
        B, S, D = x.shape
        X = (lambda x, axes=(-2,-1): torch.fft.rfft2(x.float(), dim=axes))(x, axes=(-2, -1))
        W = w_r + 1j * w_i
        X = X * W
        y = (lambda x, s, axes=(-2,-1): torch.fft.irfft2(x, s=s, dim=axes).to(torch.bfloat16))(X, (S, D), axes=(-2, -1))
        return y

    def _B_ref(x, Lambda_re, Lambda_im, B_re, B_im, C_re, C_im, log_dt, Dskip):
        Bsz, L, H = x.shape
        Lam = Lambda_re + 1j * Lambda_im
        Bmat = B_re + 1j * B_im
        Cmat = C_re + 1j * C_im
        dt = torch.exp(log_dt)
        Lam_bar = torch.exp(dt * Lam)
        B_bar = ((Lam_bar - 1.0) / Lam)[:, None] * Bmat
        Bu = torch.einsum("bld,pd->blp", x + 0j, B_bar)
        h = torch.zeros_like(Bu[:, 0, :])
        hs = []
        for t in range(L):
            h = Lam_bar[None, :] * h + Bu[:, t, :]
            hs.append(h)
        hseq = torch.stack(hs, 1)
        y = 2.0 * torch.einsum("blp,dp->bld", hseq, Cmat).real + x * Dskip[None, None, :]
        return y

    x = A_x
    g = _sigmoid((x * gate_w).sum(-1, keepdims=True))
    a = _A_ref(x, A_w_r, A_w_i)
    b = _B_ref(x, B_Lambda_re, B_Lambda_im, B_B_re, B_B_im, B_C_re, B_C_im, B_log_dt, B_Dskip)
    return g * a + (1 - g) * b
```

```cpp
#include <hip/hip_runtime.h>

typedef unsigned short u16;
typedef unsigned int u32;
typedef unsigned long long u64;
typedef __bf16 bf16x8 __attribute__((ext_vector_type(8)));
typedef __bf16 bf16x2 __attribute__((ext_vector_type(2)));
typedef short s16x4 __attribute__((ext_vector_type(4)));
typedef float f32x2 __attribute__((ext_vector_type(2)));
typedef float f32x4 __attribute__((ext_vector_type(4)));
typedef float f32x16 __attribute__((ext_vector_type(16)));
typedef u32 u32x2 __attribute__((ext_vector_type(2)));
typedef u32 u32x4 __attribute__((ext_vector_type(4)));
#define DI __device__ __forceinline__
#define LAS __attribute__((address_space(3)))
#define GAS __attribute__((address_space(1)))

namespace tab {
struct FStage { u16 v[128 * 64]; };
struct FTile { FStage s[10]; };
struct FAll { FTile t[5]; };
struct IStage { u16 v[80 * 64]; };
struct ITile { IStage s[10]; };
struct IAll { ITile t[8]; };
struct Frags { u16 v[26 * 512]; };
#if defined(__HIP_DEVICE_COMPILE__)
constexpr double PI = 3.14159265358979323846264338327950288;
constexpr double tsin(double x) { double t = x, s = x; for (int i = 1; i < 14; ++i) { t *= -x * x / ((2 * i) * (2 * i + 1)); s += t; } return s; }
constexpr double tcos(double x) { double t = 1, s = 1; for (int i = 1; i < 14; ++i) { t *= -x * x / ((2 * i - 1) * (2 * i)); s += t; } return s; }
constexpr double cosn(int i, int n) {
    i %= n; if (i < 0) i += n;
    if (2 * i > n) i = n - i;
    if (4 * i > n) return -cosn(n / 2 - i, n);
    if (8 * i > n) return tsin(2 * PI * (n / 4 - i) / n);
    return tcos(2 * PI * i / n);
}
constexpr u16 bf(double v) { float f = (float)v; u32 u = __builtin_bit_cast(u32, f); u += 0x7fffu + ((u >> 16) & 1u); return (u16)(u >> 16); }
struct C640 { u16 c[1280]; u16 c2[1280]; };
constexpr C640 mk640() { C640 r{}; for (int i = 0; i < 640; ++i) { double c = cosn(i, 640); r.c[i] = r.c[i + 640] = bf(c); r.c2[i] = r.c2[i + 640] = bf(2 * c); } return r; }
constexpr C640 c640 = mk640();
constexpr FStage mkF(int nt, int k) {
    FStage s{};
    for (int row = 0; row < 128; ++row) {
        int part = (row >> 5) & 1, col = 64 * nt + 32 * (row >> 6) + (row & 31), sw = (row >> 1) & 7;
        int step = col, ph = part ? 160 : 0;
        if (part && col == 0) { step = 320; ph = 0; }
        int idx = (step * 64 * k + ph) % 640;
        for (int ch = 0; ch < 8; ++ch) {
            u16* o = s.v + row * 64 + (ch ^ sw) * 8;
            for (int j = 0; j < 8; ++j) { o[j] = c640.c[idx]; idx += step; if (idx >= 640) idx -= 640; }
        }
    }
    return s;
}
constexpr IStage mkI(int dt, int k) {
    IStage s{};
    for (int row = 0; row < 80; ++row) {
        int d = 80 * dt + row, sw = (row >> 1) & 7;
        int idx = (d * 32 * k) % 640;
        for (int ch = 0; ch < 8; ++ch) {
            u16* o = s.v + row * 64 + (ch ^ sw) * 8;
            for (int j = 0; j < 8; j += 2) { o[j] = c640.c2[idx]; o[j + 1] = c640.c2[idx + 160]; idx += d; if (idx >= 640) idx -= 640; }
        }
        if (k == 0) { u16* o = s.v + row * 64 + sw * 8; o[0] = 0x3f80; o[1] = (d & 1) ? 0xbf80 : 0x3f80; }
    }
    return s;
}
template <int a, int k> struct FS { static constexpr FStage s = mkF(a, k); };
template <int a, int k> struct IS { static constexpr IStage s = mkI(a, k); };
template <int a> constexpr FTile mkFT() { return FTile{{FS<a, 0>::s, FS<a, 1>::s, FS<a, 2>::s, FS<a, 3>::s, FS<a, 4>::s, FS<a, 5>::s, FS<a, 6>::s, FS<a, 7>::s, FS<a, 8>::s, FS<a, 9>::s}}; }
template <int a> constexpr ITile mkIT() { return ITile{{IS<a, 0>::s, IS<a, 1>::s, IS<a, 2>::s, IS<a, 3>::s, IS<a, 4>::s, IS<a, 5>::s, IS<a, 6>::s, IS<a, 7>::s, IS<a, 8>::s, IS<a, 9>::s}}; }
constexpr Frags mkFrags() {
    Frags f{};
    for (int l = 0; l < 64; ++l)
        for (int j = 0; j < 8; ++j) {
            int m = l & 15, g = l >> 4, perm = 16 * (j >> 2) + 4 * g + (j & 3);
            for (int a = 0; a < 2; ++a) {
                int x = (8 * g + j) * (16 * a + m) * 2;
                f.v[(3 * a + 0) * 512 + l * 8 + j] = bf(cosn(x, 64));
                f.v[(3 * a + 1) * 512 + l * 8 + j] = bf(cosn(x - 16, 64));
                f.v[(3 * a + 2) * 512 + l * 8 + j] = bf(cosn(x + 16, 64));
                int r = 16 * a + m;
                f.v[(6 + 2 * a) * 512 + l * 8 + j] = bf(cosn(2 * r * perm, 64));
                f.v[(7 + 2 * a) * 512 + l * 8 + j] = bf(cosn(2 * r * perm - 16, 64));
                f.v[(10 + 2 * a) * 512 + l * 8 + j] = bf(cosn(r * (2 * perm + 1), 64));
                f.v[(11 + 2 * a) * 512 + l * 8 + j] = bf(cosn(r * (2 * perm + 1) - 16, 64));
                f.v[(14 + 2 * a) * 512 + l * 8 + j] = bf(cosn(perm * (2 * r + 1), 64));
                f.v[(15 + 2 * a) * 512 + l * 8 + j] = bf(cosn(perm * (2 * r + 1) - 16, 64));
                for (int w = 0; w < 2; ++w) {
                    int y = (16 * w + m) * (16 * a + 4 * g + (j & 3)) * 2;
                    f.v[(18 + 4 * w + 2 * a) * 512 + l * 8 + j] = bf(cosn(j < 4 ? y : y + 16, 64));
                    f.v[(19 + 4 * w + 2 * a) * 512 + l * 8 + j] = bf(cosn(j < 4 ? y - 16 : y, 64));
                }
            }
        }
    return f;
}
#endif
}
#if defined(__HIP_DEVICE_COMPILE__)
static __device__ __attribute__((aligned(1024))) const tab::FAll g_F = {{tab::mkFT<0>(), tab::mkFT<1>(), tab::mkFT<2>(), tab::mkFT<3>(), tab::mkFT<4>()}};
static __device__ __attribute__((aligned(1024))) const tab::IAll g_I = {{tab::mkIT<0>(), tab::mkIT<1>(), tab::mkIT<2>(), tab::mkIT<3>(), tab::mkIT<4>(), tab::mkIT<5>(), tab::mkIT<6>(), tab::mkIT<7>()}};
static __device__ __attribute__((aligned(1024))) const tab::Frags g_T = tab::mkFrags();
#else
static __device__ const tab::FAll g_F = {};
static __device__ const tab::IAll g_I = {};
static __device__ const tab::Frags g_T = {};
#endif

constexpr size_t WS_XF = 0;
constexpr size_t WS_H = WS_XF + 640 * 4096;
constexpr size_t WS_WR = WS_H + 16 * 1280 * 256;
constexpr size_t WS_WI = WS_WR + 321 * 4096;
constexpr size_t WS_YT = WS_WI + 321 * 4096;
constexpr size_t WS_P = WS_YT + 640 * 4096;
constexpr size_t WS_G = WS_P + 2 * 2048 * 640 * 2;
constexpr size_t WS_GR = WS_G + 2048 * 4;
constexpr size_t WS_EP = WS_GR + 10 * 16 * 128 * 8;

DI void glds16(const void* g, char* l) { __builtin_amdgcn_global_load_lds((const GAS u32*)g, (LAS u32*)l, 16, 0, 0); }
DI u32 pk2(float a, float b) { f32x2 v = {a, b}; return __builtin_bit_cast(u32, __builtin_convertvector(v, bf16x2)); }
DI float bflo(u32 u) { return __builtin_bit_cast(float, u << 16); }
DI float bfhi(u32 u) { return __builtin_bit_cast(float, u & 0xffff0000u); }
DI float bf2f(u16 u) { return __builtin_bit_cast(float, (u32)u << 16); }
DI float rbf(float a) { return bflo(pk2(a, 0.f)); }
DI bf16x8 ldsv(const char* p) { return *(const LAS bf16x8*)(p); }
DI bf16x8 asfrag(u32 a, u32 b, u32 c, u32 d) { u32x4 v = {a, b, c, d}; return __builtin_bit_cast(bf16x8, v); }
DI bf16x8 trfrag(const char* p0, const char* p1) {
    s16x4 lo = __builtin_amdgcn_ds_read_tr16_b64_v4i16((LAS s16x4*)p0);
    s16x4 hi = __builtin_amdgcn_ds_read_tr16_b64_v4i16((LAS s16x4*)p1);
    return __builtin_bit_cast(bf16x8, __builtin_shufflevector(lo, hi, 0, 1, 2, 3, 4, 5, 6, 7));
}
#define MFMA32(a, b, c) __builtin_amdgcn_mfma_f32_32x32x16_bf16((a), (b), (c), 0, 0, 0)
#define MFMA16(a, b, c) __builtin_amdgcn_mfma_f32_16x16x32_bf16((a), (b), (c), 0, 0, 0)
#define WAIT_VM(n) asm volatile("s_waitcnt vmcnt(%0)" ::"i"(n) : "memory")
#define WAIT_LGKM0() asm volatile("s_waitcnt lgkmcnt(0)" ::: "memory")
#define BAR() __builtin_amdgcn_s_barrier()
#define SCHED() __builtin_amdgcn_sched_barrier(0)
#define RLX_AGENT __ATOMIC_RELAXED, __HIP_MEMORY_SCOPE_AGENT
struct cf { float r, i; };
DI cf cmul(cf a, cf b) { return cf{a.r * b.r - a.i * b.i, a.r * b.i + a.i * b.r}; }
DI cf cfma(cf a, cf b, cf c) { return cf{fmaf(a.r, b.r, fmaf(-a.i, b.i, c.r)), fmaf(a.r, b.i, fmaf(a.i, b.r, c.i))}; }

constexpr int STG = 32768;
constexpr int LDS_X = 4 * STG;

template <class F>
DI void ring_loader(F issue) {
    issue(0); issue(1); issue(2);
#pragma unroll
    for (int st = 0; st < 10; ++st) {
        if (st <= 7) WAIT_VM(16); else if (st == 8) WAIT_VM(8); else WAIT_VM(0);
        BAR();
        if (st + 3 < 10) issue(st + 3);
    }
}

__global__ __launch_bounds__(512, 2) void k1(const u16* __restrict__ x, const u16* __restrict__ wr, const u16* __restrict__ wi,
                                             const u16* __restrict__ lre, const u16* __restrict__ lim, const u16* __restrict__ bre,
                                             const u16* __restrict__ bim, const u16* __restrict__ ldt, char* __restrict__ ws) {
    extern __shared__ __attribute__((aligned(16))) char lds[];
    const int tid = threadIdx.x, l = tid & 63, wid = __builtin_amdgcn_readfirstlane(tid >> 6);
    const int xcd = blockIdx.x & 7, j = blockIdx.x >> 3, q = xcd >> 1, s = xcd & 1;

    if (j >= 30) {
        const int job = xcd * 2 + (j - 30);
        for (int arr = 0; arr < 2; ++arr) {
            const char* src = (const char*)(arr ? wi : wr) + (size_t)job * 82176;
            for (int i = wid; i < 81; i += 8) {
                int o = i * 1024 + l * 16;
                if (o > 82176 - 16) o = 82176 - 16;
                glds16(src + o, lds + i * 1024);
            }
            WAIT_VM(0);
            __syncthreads();
            char* dst = ws + (arr ? WS_WI : WS_WR) + job * 256 + (l & 31) * 8;
            const char* p = lds + (l & 31) * 642;
            for (int c = wid * 2 + (l >> 5); c < 321; c += 16) {
                u32 v0 = *(const LAS u16*)(p + c * 2), v1 = *(const LAS u16*)(p + c * 2 + 32 * 642);
                u32 v2 = *(const LAS u16*)(p + c * 2 + 64 * 642), v3 = *(const LAS u16*)(p + c * 2 + 96 * 642);
                u32x2 o = {v0 | (v1 << 16), v2 | (v3 << 16)};
                *(GAS u32x2*)(dst + (size_t)c * 4096) = o;
            }
            __syncthreads();
        }
        return;
    }

    const bool isB = j < 20;
    int m = 0, ct = 0, mt4 = 0, nt = 0;
    if (isB) { m = 4 * q + (j & 3); ct = 5 * s + (j >> 2); }
    else { int jj = j - 20; if (jj < 8) { mt4 = jj & 3; nt = 3 * s + (jj >> 2); } else { mt4 = 2 * s + jj - 8; nt = 2; } }
    const int k0 = (j * 7 + xcd) % 10;
    float* par = (float*)(lds + LDS_X);
    float* agg = (float*)(lds + LDS_X + 1024);
    float* hp = (float*)(lds + LDS_X + 3072);

    if (wid >= 4) {
        const int w = wid - 4, rl = 8 * w + (l >> 3), sw = (4 * w + (l >> 4)) & 7, cs = ((l & 7) ^ sw) * 16;
        const char *pa, *pb0, *pb1; int sa, sb, kb;
        if (isB) {
            pa = (const char*)x + (size_t)(128 * m + rl) * 1280 + cs; sa = 32 * 1280;
            pb0 = (const char*)bre + (size_t)(64 * ct + rl) * 1280 + cs; pb1 = (const char*)bim + (size_t)(64 * ct + rl) * 1280 + cs;
            sb = 32 * 1280; kb = 128;
        } else {
            pa = (const char*)x + (size_t)(64 * (8 * q + (rl & 7)) + 32 * (mt4 & 1) + 2 * (rl >> 3) + (mt4 >> 1)) * 1280 + cs; sa = 8 * 1280;
            pb0 = (const char*)&g_F + (size_t)nt * 163840 + w * 1024 + l * 16; pb1 = pb0 + 4096; sb = 8192; kb = 16384;
        }
        GAS u32* ep = (GAS u32*)(ws + WS_EP + ct * 256);
        const u32 tag = __hip_atomic_load(ep, RLX_AGENT) * 1664525u + 1013904223u;
        auto issue = [&](int st) {
            int kk = st + k0; if (kk >= 10) kk -= 10;
            char* d = lds + (st & 3) * STG + w * 1024;
#pragma unroll
            for (int i = 0; i < 4; ++i) glds16(pa + i * sa + kk * 128, d + i * 4096);
            glds16(pb0 + kk * kb, d + 16384); glds16(pb1 + kk * kb, d + 20480);
            glds16(pb0 + sb + kk * kb, d + 24576); glds16(pb1 + sb + kk * kb, d + 28672);
        };
        ring_loader(issue);
        if (!isB) { BAR(); return; }
        BAR();
        cf l32 = {par[l * 4], par[l * 4 + 1]}, l128 = {par[l * 4 + 2], par[l * 4 + 3]};
        GAS u64* gr = (GAS u64*)(ws + WS_GR) + (size_t)ct * 2048 + l * 2;
        if (w == 0 && m < 15) {
            cf e = {agg[l * 2], agg[l * 2 + 1]};
#pragma unroll
            for (int sg = 1; sg < 4; ++sg) e = cfma(l32, e, cf{agg[sg * 128 + l * 2], agg[sg * 128 + l * 2 + 1]});
            __hip_atomic_store(gr + m * 128, ((u64)tag << 32) | __builtin_bit_cast(u32, e.r), RLX_AGENT);
            __hip_atomic_store(gr + m * 128 + 1, ((u64)tag << 32) | __builtin_bit_cast(u32, e.i), RLX_AGENT);
        }
        cf l256 = cmul(l128, l128), l512 = cmul(l256, l256);
        cf lw = {1.f, 0.f};
        if (w & 1) lw = l128;
        if (w & 2) lw = cmul(lw, l256);
        cf ev[4];
        for (int spin = 0; spin < (1 << 16); ++spin) {
            bool ok = true;
#pragma unroll
            for (int i = 0; i < 4; ++i) {
                int mp = m - 1 - w - 4 * i;
                ev[i] = cf{0.f, 0.f};
                if (mp >= 0) {
                    u64 a = __hip_atomic_load(gr + mp * 128, RLX_AGENT), b = __hip_atomic_load(gr + mp * 128 + 1, RLX_AGENT);
                    ok &= (u32)(a >> 32) == tag && (u32)(b >> 32) == tag;
                    ev[i] = cf{__builtin_bit_cast(float, (u32)a), __builtin_bit_cast(float, (u32)b)};
                }
            }
            if (__all(ok)) break;
            __builtin_amdgcn_s_sleep(1);
        }
        cf h = cmul(lw, cfma(l512, cfma(l512, cfma(l512, ev[3], ev[2]), ev[1]), ev[0]));
        hp[w * 128 + l * 2] = h.r; hp[w * 128 + l * 2 + 1] = h.i;
        WAIT_LGKM0();
        BAR();
        if (m == 15 && w == 0 && l == 0) __hip_atomic_store(ep, tag, RLX_AGENT);
        return;
    }

    const int wm = wid >> 1, wn = wid & 1, r = l & 31, hh = l >> 5;
    cf lam = {0.f, 0.f}, coef = {0.f, 0.f}, l32 = {0.f, 0.f};
    if (isB) {
        int p = 64 * ct + 32 * wn + r;
        float ar = bf2f(lre[p]), ai = bf2f(lim[p]), dt = rbf(expf(bf2f(ldt[p])));
        float ex = expf(dt * ar), sn, cs;
        sincosf(dt * ai, &sn, &cs);
        lam = cf{ex * cs, ex * sn};
        float den = 1.f / (ar * ar + ai * ai), nr = lam.r - 1.f;
        coef = cf{(nr * ar + lam.i * ai) * den, (lam.i * ar - nr * ai) * den};
        l32 = lam;
#pragma unroll
        for (int i = 0; i < 5; ++i) l32 = cmul(l32, l32);
        cf l64 = cmul(l32, l32), l128 = cmul(l64, l64);
        if (wm == 0 && hh == 0) { f32x4 v = {l32.r, l32.i, l128.r, l128.i}; *(LAS f32x4*)(par + (32 * wn + r) * 4) = v; }
    }
    f32x16 acc[2][2];
#pragma unroll
    for (int i = 0; i < 16; ++i) { acc[0][0][i] = 0.f; acc[0][1][i] = 0.f; acc[1][0][i] = 0.f; acc[1][1][i] = 0.f; }
    {
        const int rowA = 64 * wm + 32 * ((r >> 2) & 1) + 4 * (r >> 3) + (r & 3), swA = (rowA >> 1) & 7;
        const int rowB = 64 * wn + r, swB = (r >> 1) & 7;
        int aa[4], ba[4];
#pragma unroll
        for (int ks = 0; ks < 4; ++ks) { aa[ks] = rowA * 128 + ((hh ^ swA ^ (2 * ks)) * 16); ba[ks] = 16384 + rowB * 128 + ((hh ^ swB ^ (2 * ks)) * 16); }
        bf16x8 fa[2][2], fb[2][2];
#pragma unroll
        for (int st = 0; st < 10; ++st) {
            WAIT_LGKM0();
            BAR();
            const char* sp = lds + (st & 3) * STG;
#pragma unroll
            for (int ks = 0; ks < 4; ++ks) {
                const int c = ks & 1;
                fa[c][0] = ldsv(sp + aa[ks]); fb[c][0] = ldsv(sp + ba[ks]);
                fa[c][1] = ldsv(sp + aa[ks] + 2048); fb[c][1] = ldsv(sp + ba[ks] + 4096);
                SCHED();
                if (st + ks > 0) {
                    acc[0][0] = MFMA32(fa[c ^ 1][0], fb[c ^ 1][0], acc[0][0]); acc[1][0] = MFMA32(fa[c ^ 1][1], fb[c ^ 1][0], acc[1][0]);
                    acc[0][1] = MFMA32(fa[c ^ 1][0], fb[c ^ 1][1], acc[0][1]); acc[1][1] = MFMA32(fa[c ^ 1][1], fb[c ^ 1][1], acc[1][1]);
                }
                SCHED();
            }
        }
        acc[0][0] = MFMA32(fa[1][0], fb[1][0], acc[0][0]); acc[1][0] = MFMA32(fa[1][1], fb[1][0], acc[1][0]);
        acc[0][1] = MFMA32(fa[1][0], fb[1][1], acc[0][1]); acc[1][1] = MFMA32(fa[1][1], fb[1][1], acc[1][1]);
    }
    char* ob = lds + 2 * STG + wid * 8192;
    if (isB) {
        cf e = {0.f, 0.f};
#pragma unroll
        for (int t = 0; t < 32; ++t) e = cfma(lam, e, cf{acc[t >> 4][0][t & 15], acc[t >> 4][1][t & 15]});
        const int seg = 2 * wm + hh, ch = 32 * wn + r;
        { f32x2 v = {e.r, e.i}; *(LAS f32x2*)(agg + seg * 128 + ch * 2) = v; }
        WAIT_LGKM0();
        BAR();
        BAR();
        cf c = {0.f, 0.f};
#pragma unroll
        for (int w = 0; w < 4; ++w) { f32x2 v = *(const LAS f32x2*)(hp + w * 128 + ch * 2); c.r += v[0]; c.i += v[1]; }
#pragma unroll
        for (int sg = 0; sg < 3; ++sg) {
            f32x2 v = *(const LAS f32x2*)(agg + sg * 128 + ch * 2);
            cf n = cfma(l32, c, cf{v[0], v[1]});
            if (sg < seg) c = n;
        }
#pragma unroll
        for (int t = 0; t < 32; ++t) {
            c = cfma(lam, c, cf{acc[t >> 4][0][t & 15], acc[t >> 4][1][t & 15]});
            acc[t >> 4][0][t & 15] = coef.r * c.r - coef.i * c.i;
            acc[t >> 4][1][t & 15] = -(coef.r * c.i + coef.i * c.r);
        }
#pragma unroll
        for (int part = 0; part < 2; ++part)
#pragma unroll
            for (int e2 = 0; e2 < 2; ++e2)
#pragma unroll
                for (int g = 0; g < 4; ++g) {
                    const int t = 8 * g + e2;
                    u32x2 v = {pk2(acc[t >> 4][part][t & 15], acc[t >> 4][part][(t & 15) + 2]), pk2(acc[t >> 4][part][(t & 15) + 4], acc[t >> 4][part][(t & 15) + 6])};
                    *(LAS u32x2*)(ob + (32 * part + r) * 128 + (((4 * e2 + g) ^ (r & 7)) * 16) + hh * 8) = v;
                }
    } else {
        BAR();
#pragma unroll
        for (int part = 0; part < 2; ++part)
#pragma unroll
            for (int i = 0; i < 4; ++i) {
                const f32x16& a = acc[i >> 1][part];
                const int o = (i & 1) * 8;
                u32x4 v = {pk2(a[o], a[o + 1]), pk2(a[o + 2], a[o + 3]), pk2(a[o + 4], a[o + 5]), pk2(a[o + 6], a[o + 7])};
                *(LAS u32x4*)(ob + (32 * part + r) * 128 + (((4 * hh + i) ^ (r & 7)) * 16)) = v;
            }
    }
    char* gd; int rstride, pstride;
    if (isB) { gd = ws + WS_H + (size_t)(m * 10 + ct) * 32768 + (32 * wn) * 256 + wm * 128; rstride = 256; pstride = 16384; }
    else { gd = ws + WS_XF + (size_t)(64 * nt + 32 * wn) * 8192 + (mt4 * 4 + q) * 256 + wm * 128; rstride = 8192; pstride = 4096; }
    gd += (l >> 3) * rstride + (l & 7) * 16;
    const char* rd = ob + (l >> 3) * 128 + (((l & 7) ^ (l >> 3)) * 16);
#pragma unroll
    for (int i = 0; i < 8; ++i) {
        u32x4 v = *(const LAS u32x4*)(rd + i * 1024);
        *(GAS u32x4*)(gd + (i >> 2) * pstride + (i & 3) * 8 * rstride) = v;
    }
}

DI void fft_job(char* lds, char* ws, int f, int wid, int l) {
    for (int i = wid; i < 26; i += 8) glds16((const char*)&g_T + i * 1024 + l * 16, lds + i * 1024);
    const int jb = 4 * f + (wid >> 1), a = wid & 1, mm = l & 15, g = l >> 4;
    const bool act = jb <= 320, realonly = jb == 0 || jb == 320;
    const int col = act ? jb : 0, xb = jb == 320 ? 1 : 2 * col;
    bf16x8 xr[4], xi[4];
    u32x2 wre[4], wim[4];
    {
        const char* xp = ws + WS_XF + (size_t)xb * 4096 + l * 16;
#pragma unroll
        for (int t = 0; t < 4; ++t) xr[t] = *(const GAS bf16x8*)(xp + t * 1024);
#pragma unroll
        for (int t = 0; t < 4; ++t) xi[t] = *(const GAS bf16x8*)(xp + (realonly ? 0 : 4096) + t * 1024);
        const char* wp = ws + (size_t)col * 4096 + g * 256 + (16 * a + mm) * 8;
#pragma unroll
        for (int t = 0; t < 4; ++t) { wre[t] = *(const GAS u32x2*)(wp + WS_WR + t * 1024); wim[t] = *(const GAS u32x2*)(wp + WS_WI + t * 1024); }
    }
    float tc[16], ts[16], uc[16], us[16];
#pragma unroll
    for (int i = 0; i < 16; ++i) {
        const int e = i >> 3, u = (i >> 2) & 1, c = i & 3;
        float f1 = (float)((2 * (16 * u + 4 * g + c) + e) * (16 * a + mm)) * (1.f / 2048.f);
        float f2 = (float)((2 * (16 * u + mm) + e) * (16 * a + 4 * g + c)) * (1.f / 2048.f);
        tc[i] = __builtin_amdgcn_cosf(f1); ts[i] = __builtin_amdgcn_sinf(f1);
        uc[i] = __builtin_amdgcn_cosf(f2); us[i] = __builtin_amdgcn_sinf(f2);
    }
    WAIT_VM(0);
    BAR();
    if (realonly) {
#pragma unroll
        for (int t = 0; t < 4; ++t) xi[t] = asfrag(0, 0, 0, 0);
    }
    const char* tp = lds + l * 16;
    const f32x4 z4 = {0.f, 0.f, 0.f, 0.f};
    u32 pr[2][4], pi[2][4], pn[2][4];
    {
        bf16x8 C = ldsv(tp + (3 * a) * 1024), S = ldsv(tp + (3 * a + 1) * 1024), N = ldsv(tp + (3 * a + 2) * 1024);
#pragma unroll
        for (int t = 0; t < 4; ++t) {
            f32x4 yr = MFMA16(xi[t], S, MFMA16(xr[t], C, z4));
            f32x4 yi = MFMA16(xr[t], N, MFMA16(xi[t], C, z4));
            float a_[4], b_[4];
#pragma unroll
            for (int c = 0; c < 4; ++c) { a_[c] = yr[c] * tc[4 * t + c] + yi[c] * ts[4 * t + c]; b_[c] = yi[c] * tc[4 * t + c] - yr[c] * ts[4 * t + c]; }
            const int e = t >> 1, u = t & 1;
            pr[e][2 * u] = pk2(a_[0], a_[1]); pr[e][2 * u + 1] = pk2(a_[2], a_[3]);
            pi[e][2 * u] = pk2(b_[0], b_[1]); pi[e][2 * u + 1] = pk2(b_[2], b_[3]);
            pn[e][2 * u] = pr[e][2 * u] ^ 0x80008000u; pn[e][2 * u + 1] = pr[e][2 * u + 1] ^ 0x80008000u;
        }
    }
    bf16x8 EC[2], ES[2];
    u32 qr[2][4], qi[2][4], qn[2][4];
#pragma unroll
    for (int v = 0; v < 2; ++v) {
        EC[v] = ldsv(tp + (6 + 2 * v) * 1024); ES[v] = ldsv(tp + (7 + 2 * v) * 1024);
        bf16x8 OC = ldsv(tp + (10 + 2 * v) * 1024), OS = ldsv(tp + (11 + 2 * v) * 1024);
        bf16x8 t0r = asfrag(pr[0][0], pr[0][1], pr[0][2], pr[0][3]), t0i = asfrag(pi[0][0], pi[0][1], pi[0][2], pi[0][3]), t0n = asfrag(pn[0][0], pn[0][1], pn[0][2], pn[0][3]);
        bf16x8 t1r = asfrag(pr[1][0], pr[1][1], pr[1][2], pr[1][3]), t1i = asfrag(pi[1][0], pi[1][1], pi[1][2], pi[1][3]), t1n = asfrag(pn[1][0], pn[1][1], pn[1][2], pn[1][3]);
        f32x4 er = MFMA16(ES[v], t0i, MFMA16(EC[v], t0r, z4)), ei = MFMA16(ES[v], t0n, MFMA16(EC[v], t0i, z4));
        f32x4 dr = MFMA16(OS, t1i, MFMA16(OC, t1r, z4)), di = MFMA16(OS, t1n, MFMA16(OC, t1i, z4));
        float p0r[4], p0i[4], p1r[4], p1i[4];
#pragma unroll
        for (int c = 0; c < 4; ++c) {
            u32 a0 = wre[v][c >> 1], b0 = wim[v][c >> 1], a1 = wre[2 + v][c >> 1], b1 = wim[2 + v][c >> 1];
            cf wl = (c & 1) ? cf{bfhi(a0), bfhi(b0)} : cf{bflo(a0), bflo(b0)};
            cf wh = (c & 1) ? cf{bfhi(a1), bfhi(b1)} : cf{bflo(a1), bflo(b1)};
            cf lo = cmul(cf{er[c] + dr[c], ei[c] + di[c]}, wl), hi = cmul(cf{er[c] - dr[c], ei[c] - di[c]}, wh);
            p0r[c] = lo.r + hi.r; p0i[c] = lo.i + hi.i; p1r[c] = lo.r - hi.r; p1i[c] = lo.i - hi.i;
        }
        qr[0][2 * v] = pk2(p0r[0], p0r[1]); qr[0][2 * v + 1] = pk2(p0r[2], p0r[3]);
        qi[0][2 * v] = pk2(p0i[0], p0i[1]); qi[0][2 * v + 1] = pk2(p0i[2], p0i[3]);
        qr[1][2 * v] = pk2(p1r[0], p1r[1]); qr[1][2 * v + 1] = pk2(p1r[2], p1r[3]);
        qi[1][2 * v] = pk2(p1i[0], p1i[1]); qi[1][2 * v + 1] = pk2(p1i[2], p1i[3]);
    }
#pragma unroll
    for (int e = 0; e < 2; ++e)
#pragma unroll
        for (int i = 0; i < 4; ++i) qn[e][i] = qi[e][i] ^ 0x80008000u;
    u32x4 vf[2][2];
#pragma unroll
    for (int e = 0; e < 2; ++e) {
        bf16x8 ar = asfrag(qr[e][0], qr[e][1], qr[e][2], qr[e][3]), ai = asfrag(qi[e][0], qi[e][1], qi[e][2], qi[e][3]), an = asfrag(qn[e][0], qn[e][1], qn[e][2], qn[e][3]);
#pragma unroll
        for (int u = 0; u < 2; ++u) {
            bf16x8 C = e ? ldsv(tp + (14 + 2 * u) * 1024) : EC[u], S = e ? ldsv(tp + (15 + 2 * u) * 1024) : ES[u];
            f32x4 vr = MFMA16(an, S, MFMA16(ar, C, z4)), vi = MFMA16(ar, S, MFMA16(ai, C, z4));
            float a_[4], b_[4];
            const int t = 2 * e + u;
#pragma unroll
            for (int c = 0; c < 4; ++c) { a_[c] = vr[c] * uc[4 * t + c] - vi[c] * us[4 * t + c]; b_[c] = vi[c] * uc[4 * t + c] + vr[c] * us[4 * t + c]; }
            vf[e][u] = u32x4{pk2(a_[0], a_[1]), pk2(a_[2], a_[3]), pk2(b_[0], b_[1]), pk2(b_[2], b_[3])};
        }
    }
    char* xch = lds + 26 * 1024;
    const u32 am = a ? 0xffffffffu : 0u;
#pragma unroll
    for (int u = 0; u < 2; ++u) *(LAS u32x4*)(xch + wid * 2048 + u * 1024 + l * 16) = (vf[0][u] & am) | (vf[1][u] & ~am);
    WAIT_LGKM0();
    BAR();
    bf16x8 own[2], oth[2];
#pragma unroll
    for (int u = 0; u < 2; ++u) { own[u] = __builtin_bit_cast(bf16x8, (vf[1][u] & am) | (vf[0][u] & ~am)); oth[u] = ldsv(xch + (wid ^ 1) * 2048 + u * 1024 + l * 16); }
    char* yp = ws + WS_YT + (size_t)xb * 4096 + mm * 128 + a * 64 + g * 16;
#pragma unroll
    for (int w = 0; w < 2; ++w)
#pragma unroll
        for (int part = 0; part < 2; ++part) {
            bf16x8 Bo = ldsv(tp + (18 + 4 * w + 2 * a + part) * 1024), Bp = ldsv(tp + (18 + 4 * w + 2 * (a ^ 1) + part) * 1024);
            f32x4 o0 = MFMA16(oth[0], Bp, MFMA16(own[0], Bo, z4)), o1 = MFMA16(oth[1], Bp, MFMA16(own[1], Bo, z4));
            u32x4 v = {pk2(o0[0], o0[1]), pk2(o0[2], o0[3]), pk2(o1[0], o1[1]), pk2(o1[2], o1[3])};
            if (act && !(realonly && part)) *(GAS u32x4*)(yp + part * 4096 + w * 2048) = v;
        }
}

__global__ __launch_bounds__(512, 2) void k2(const u16* __restrict__ x, const u16* __restrict__ cre, const u16* __restrict__ cim,
                                             const u16* __restrict__ gw, char* __restrict__ ws) {
    extern __shared__ __attribute__((aligned(16))) char lds[];
    const int tid = threadIdx.x, l = tid & 63, wid = __builtin_amdgcn_readfirstlane(tid >> 6);
    const int xcd = blockIdx.x & 7, j = blockIdx.x >> 3, q = xcd >> 1, s = xcd & 1;

    if (j >= 20 && j < 30) { fft_job(lds, ws, (j - 20) * 8 + xcd, wid, l); return; }
    if (j == 30) { if (xcd == 0) fft_job(lds, ws, 80, wid, l); return; }
    if (j == 31) {
        const int l16 = l & 15;
        float gv[40];
#pragma unroll
        for (int i = 0; i < 5; ++i) {
            u32x4 v = *(const GAS u32x4*)((const char*)gw + (l16 + 16 * i) * 16);
#pragma unroll
            for (int k = 0; k < 4; ++k) { gv[i * 8 + 2 * k] = bflo(v[k]); gv[i * 8 + 2 * k + 1] = bfhi(v[k]); }
        }
        const int t0 = xcd * 256 + wid * 32 + (l >> 4);
#pragma unroll
        for (int hb = 0; hb < 2; ++hb) {
            u32x4 xv[4][5];
#pragma unroll
            for (int it = 0; it < 4; ++it)
#pragma unroll
                for (int i = 0; i < 5; ++i) xv[it][i] = *(const GAS u32x4*)((const char*)x + (size_t)(t0 + 16 * hb + 4 * it) * 1280 + (l16 + 16 * i) * 16);
#pragma unroll
            for (int it = 0; it < 4; ++it) {
                float sum = 0.f;
#pragma unroll
                for (int i = 0; i < 5; ++i)
#pragma unroll
                    for (int k = 0; k < 4; ++k) {
                        u32 p = pk2(bflo(xv[it][i][k]) * gv[i * 8 + 2 * k], bfhi(xv[it][i][k]) * gv[i * 8 + 2 * k + 1]);
                        sum += bflo(p) + bfhi(p);
                    }
                sum += __shfl_xor(sum, 1); sum += __shfl_xor(sum, 2); sum += __shfl_xor(sum, 4); sum += __shfl_xor(sum, 8);
                float gt = rbf(1.f / (1.f + expf(-rbf(sum))));
                const int t = t0 + 16 * hb + 4 * it, n2 = t & 63, np = n2 >> 1;
                const int pos = 32 * (n2 & 1) + 8 * ((np >> 2) & 3) + 4 * (np >> 4) + (np & 3);
                if (l16 == 0) *(GAS float*)(ws + WS_G + ((t & ~63) + pos) * 4) = gt;
            }
        }
        return;
    }
    const int m = 4 * q + (j & 3), nt = j >> 2;
    const int k0 = (j * 7 + xcd) % 10;
    if (wid >= 4) {
        const int w = wid - 4;
        const char* pa = ws + WS_H + (size_t)(m * 10 + 5 * s) * 32768 + (4 * w + (l >> 4)) * 256 + (((l & 15) ^ (((l >> 4) << 2) | w)) * 16);
        const int rl = 8 * w + (l >> 3), sw = (4 * w + (l >> 4)) & 7;
        const size_t bo = (size_t)(128 * nt + rl) * 1280 + 5 * s * 128 + ((l & 7) ^ sw) * 16;
        const char *pb0 = (const char*)cre + bo, *pb1 = (const char*)cim + bo;
        auto issue = [&](int st) {
            int kk = st + k0; if (kk >= 10) kk -= 10;
            char* d = lds + (st & 3) * STG + w * 1024;
            const char* pb = ((kk & 1) ? pb1 : pb0) + (kk >> 1) * 128;
#pragma unroll
            for (int i = 0; i < 4; ++i) glds16(pa + kk * 16384 + i * 4096, d + i * 4096);
#pragma unroll
            for (int i = 0; i < 4; ++i) glds16(pb + i * 32 * 1280, d + 16384 + i * 4096);
        };
        ring_loader(issue);
        return;
    }
    const int wm = wid >> 1, wn = wid & 1, r = l & 31, hh = l >> 5;
    f32x16 acc[2][2];
#pragma unroll
    for (int i = 0; i < 16; ++i) { acc[0][0][i] = 0.f; acc[0][1][i] = 0.f; acc[1][0][i] = 0.f; acc[1][1][i] = 0.f; }
    {
        const int blk = (l >> 4) & 1, qq = (l & 15) >> 2, p = l & 3;
        int aa[2][2], ba[4];
#pragma unroll
        for (int mt = 0; mt < 2; ++mt)
#pragma unroll
            for (int t = 0; t < 2; ++t)
                aa[mt][t] = 256 * (8 * hh + 4 * t + qq) + 16 * ((4 * (2 * wm + mt) + 2 * blk + (p >> 1)) ^ ((qq << 2) | ((2 * hh + t) & 3))) + 8 * (p & 1);
        const int rowB = 64 * wn + r, swB = (r >> 1) & 7;
#pragma unroll
        for (int ks = 0; ks < 4; ++ks) ba[ks] = 16384 + rowB * 128 + ((hh ^ swB ^ (2 * ks)) * 16);
        bf16x8 fa[2][2], fb[2][2];
#pragma unroll
        for (int st = 0; st < 10; ++st) {
            WAIT_LGKM0();
            BAR();
            const char* sp = lds + (st & 3) * STG;
#pragma unroll
            for (int ks = 0; ks < 4; ++ks) {
                const int c = ks & 1;
                fa[c][0] = trfrag(sp + aa[0][0] + ks * 4096, sp + aa[0][1] + ks * 4096); fb[c][0] = ldsv(sp + ba[ks]);
                fa[c][1] = trfrag(sp + aa[1][0] + ks * 4096, sp + aa[1][1] + ks * 4096); fb[c][1] = ldsv(sp + ba[ks] + 4096);
                SCHED();
                if (st + ks > 0) {
                    acc[0][0] = MFMA32(fa[c ^ 1][0], fb[c ^ 1][0], acc[0][0]); acc[1][0] = MFMA32(fa[c ^ 1][1], fb[c ^ 1][0], acc[1][0]);
                    acc[0][1] = MFMA32(fa[c ^ 1][0], fb[c ^ 1][1], acc[0][1]); acc[1][1] = MFMA32(fa[c ^ 1][1], fb[c ^ 1][1], acc[1][1]);
                }
                SCHED();
            }
        }
        acc[0][0] = MFMA32(fa[1][0], fb[1][0], acc[0][0]); acc[1][0] = MFMA32(fa[1][1], fb[1][0], acc[1][0]);
        acc[0][1] = MFMA32(fa[1][0], fb[1][1], acc[0][1]); acc[1][1] = MFMA32(fa[1][1], fb[1][1], acc[1][1]);
    }
    char* pd = ws + WS_P + ((size_t)((s * 32 + 2 * m + wm) * 16 + hh) * 640 + 128 * nt + 64 * wn + r) * 8;
#pragma unroll
    for (int mt = 0; mt < 2; ++mt)
#pragma unroll
        for (int n2 = 0; n2 < 2; ++n2)
#pragma unroll
            for (int rg = 0; rg < 4; ++rg) {
                const f32x16& a = acc[mt][n2];
                u32x2 v = {pk2(2.f * a[4 * rg], 2.f * a[4 * rg + 1]), pk2(2.f * a[4 * rg + 2], 2.f * a[4 * rg + 3])};
                *(GAS u32x2*)(pd + ((8 * mt + 2 * rg) * 640 + 32 * n2) * 8) = v;
            }
}

constexpr int S3 = 18432, NS3 = 6, X3 = NS3 * S3;
template <int PER>
DI void k3_loader(char* lds, const char* pa, const char* pb, int w, int k0) {
    auto issue = [&](int st) {
        int kk = st + k0; if (kk >= 10) kk -= 10;
        char* d = lds + (st % NS3) * S3 + w * 1024;
        glds16(pa + (size_t)kk * 64 * 4096, d); glds16(pa + (size_t)kk * 64 * 4096 + 32 * 4096, d + 4096);
#pragma unroll
        for (int i = 0; i < PER - 2; ++i) glds16(pb + kk * 10240 + i * 4096, d + 8192 + i * 4096);
    };
#pragma unroll
    for (int st = 0; st < NS3 - 1; ++st) issue(st);
#pragma unroll
    for (int st = 0; st < 10; ++st) {
        const int ahead = (9 - st) < (NS3 - 2) ? (9 - st) : (NS3 - 2);
        WAIT_VM(ahead * PER);
        BAR();
        if (st + NS3 - 1 < 10) issue(st + NS3 - 1);
    }
}

__global__ __launch_bounds__(512, 2) void k3(const u16* __restrict__ x, const u16* __restrict__ dsk, const char* __restrict__ ws, float* __restrict__ out) {
    extern __shared__ __attribute__((aligned(16))) char lds[];
    const int tid = threadIdx.x, l = tid & 63, wid = __builtin_amdgcn_readfirstlane(tid >> 6);
    const int xcd = blockIdx.x & 7, j = blockIdx.x >> 3, q = xcd >> 1, s = xcd & 1;
    const int tt = 8 * q + (j & 7), dt = 4 * s + (j >> 3);
    const int k0 = (j * 7 + xcd) % 10;
    if (wid >= 4) {
        const int w = wid - 4;
        for (int i = w; i < 10; i += 4) {
            int id = 64 * i + l, row = id / 10, ch = id - row * 10;
            glds16((const char*)x + (size_t)(64 * tt + row) * 1280 + dt * 160 + ch * 16, lds + X3 + i * 1024);
        }
        const int row = 8 * w + (l >> 3), sw = ((row >> 1) & 1) | (((row >> 3) & 1) << 1);
        const char* pa = ws + WS_YT + (size_t)row * 4096 + tt * 128 + (((l & 7) ^ (sw << 1)) * 16);
        const char* pb = (const char*)&g_I + (size_t)dt * 102400 + w * 1024 + l * 16;
        if (w < 2) k3_loader<5>(lds, pa, pb, w, k0); else k3_loader<4>(lds, pa, pb, w, k0);
        return;
    }
    const int dl = l & 15, g = l >> 4;
    u32x2 p0[5], p1[5]; u16 dk[5];
    {
        const char* pp = ws + WS_P + ((size_t)(tt * 16 + 4 * wid + g) * 640 + 80 * dt + dl) * 8;
#pragma unroll
        for (int n = 0; n < 5; ++n) { p0[n] = *(const GAS u32x2*)(pp + n * 128); p1[n] = *(const GAS u32x2*)(pp + (size_t)32 * 16 * 640 * 8 + n * 128); dk[n] = dsk[80 * dt + 16 * n + dl]; }
    }
    const f32x4 gt = *(const GAS f32x4*)(ws + WS_G + (tt * 64 + 16 * wid + 4 * g) * 4);
    f32x4 acc[5];
#pragma unroll
    for (int n = 0; n < 5; ++n) acc[n] = f32x4{0.f, 0.f, 0.f, 0.f};
    {
        const int qq = dl >> 2, p = l & 3;
        int aa[2], ba[2];
#pragma unroll
        for (int t = 0; t < 2; ++t) {
            const int row = 8 * g + 4 * t + qq, sw = ((row >> 1) & 1) | (((row >> 3) & 1) << 1);
            aa[t] = row * 128 + (((2 * wid + (p >> 1)) ^ (sw << 1)) * 16) + 8 * (p & 1);
        }
#pragma unroll
        for (int ks = 0; ks < 2; ++ks) ba[ks] = 8192 + dl * 128 + (((4 * ks + g) ^ (dl >> 1)) * 16);
        bf16x8 fa[2], fb[2][5];
#pragma unroll
        for (int st = 0; st < 10; ++st) {
            WAIT_LGKM0();
            BAR();
            const char* sp = lds + (st % NS3) * S3;
#pragma unroll
            for (int ks = 0; ks < 2; ++ks) {
                fa[ks] = trfrag(sp + aa[0] + ks * 4096, sp + aa[1] + ks * 4096);
#pragma unroll
                for (int n = 0; n < 5; ++n) fb[ks][n] = ldsv(sp + ba[ks] + n * 2048);
                SCHED();
                if (st + ks > 0) {
#pragma unroll
                    for (int n = 0; n < 5; ++n) acc[n] = MFMA16(fa[ks ^ 1], fb[ks ^ 1][n], acc[n]);
                }
                SCHED();
            }
        }
#pragma unroll
        for (int n = 0; n < 5; ++n) acc[n] = MFMA16(fa[1], fb[1][n], acc[n]);
    }
    const int i0 = 16 * wid + 4 * g;
    const int n20 = 2 * (16 * ((i0 >> 2) & 1) + 4 * ((i0 >> 3) & 3)) + (i0 >> 5);
    const char* xs = lds + X3 + n20 * 160 + dl * 2;
    float* op = out + (size_t)(64 * tt + n20) * 640 + 80 * dt + dl;
#pragma unroll
    for (int c = 0; c < 4; ++c) {
        const float gg = gt[c], og = rbf(1.f - gg);
#pragma unroll
        for (int n = 0; n < 5; ++n) {
            float a = rbf(acc[n][c] * (1.f / (2048.f * 640.f)));
            float xd = rbf(bf2f(*(const LAS u16*)(xs + c * 320 + n * 32)) * bf2f(dk[n]));
            u32 a0 = p0[n][c >> 1], a1 = p1[n][c >> 1];
            float y = (c & 1) ? bfhi(a0) + bfhi(a1) : bflo(a0) + bflo(a1);
            op[c * 1280 + n * 16] = rbf(gg * a) + og * (y + xd);
        }
    }
}

extern "C" void kernel_launch(void* const* d_in, const int* in_sizes, int n_in, void* d_out, int out_size, void* d_ws, size_t ws_size,
                              hipStream_t stream) {
    const u16* x = (const u16*)d_in[0];
    constexpr int L1 = LDS_X + 5120, L2 = LDS_X, L3 = X3 + 10240;
    (void)hipFuncSetAttribute((const void*)k1, hipFuncAttributeMaxDynamicSharedMemorySize, L1);
    (void)hipFuncSetAttribute((const void*)k2, hipFuncAttributeMaxDynamicSharedMemorySize, L2);
    (void)hipFuncSetAttribute((const void*)k3, hipFuncAttributeMaxDynamicSharedMemorySize, L3);
    k1<<<256, 512, L1, stream>>>(x, (const u16*)d_in[1], (const u16*)d_in[2], (const u16*)d_in[4], (const u16*)d_in[5], (const u16*)d_in[6],
                                 (const u16*)d_in[7], (const u16*)d_in[10], (char*)d_ws);
    k2<<<256, 512, L2, stream>>>(x, (const u16*)d_in[8], (const u16*)d_in[9], (const u16*)d_in[12], (char*)d_ws);
    k3<<<256, 512, L3, stream>>>(x, (const u16*)d_in[11], (const char*)d_ws, (float*)d_out);
}
```

```cpp
#include <hip/hip_runtime.h>

typedef unsigned short u16;
typedef unsigned int u32;
typedef unsigned long long u64;
typedef __bf16 bf16x8 __attribute__((ext_vector_type(8)));
typedef __bf16 bf16x2 __attribute__((ext_vector_type(2)));
typedef short s16x4 __attribute__((ext_vector_type(4)));
typedef float f32x2 __attribute__((ext_vector_type(2)));
typedef float f32x4 __attribute__((ext_vector_type(4)));
typedef u32 u32x2 __attribute__((ext_vector_type(2)));
typedef u32 u32x4 __attribute__((ext_vector_type(4)));
#define DI __device__ __forceinline__
#define LAS __attribute__((address_space(3)))
#define GAS __attribute__((address_space(1)))

namespace tab {
struct Frag { u16 v[512]; };
struct F50 { Frag f[50]; };
struct F32 { Frag f[32]; };
#if defined(__HIP_DEVICE_COMPILE__)
constexpr double PI = 3.14159265358979323846264338327950288;
constexpr double tsin(double x) { double t = x, s = x; for (int i = 1; i < 14; ++i) { t *= -x * x / ((2 * i) * (2 * i + 1)); s += t; } return s; }
constexpr double tcos(double x) { double t = 1, s = 1; for (int i = 1; i < 14; ++i) { t *= -x * x / ((2 * i - 1) * (2 * i)); s += t; } return s; }
constexpr double cosn(int i, int n) {
    i %= n; if (i < 0) i += n;
    if (2 * i > n) i = n - i;
    if (4 * i > n) return -cosn(n / 2 - i, n);
    if (8 * i > n) return tsin(2 * PI * (n / 4 - i) / n);
    return tcos(2 * PI * i / n);
}
constexpr u16 bf(double v) { float f = (float)v; u32 u = __builtin_bit_cast(u32, f); u += 0x7fffu + ((u >> 16) & 1u); return (u16)(u >> 16); }
struct CT { u16 c[160], c2[160], e[64]; };
constexpr CT mkct() { CT r{}; for (int i = 0; i < 160; ++i) { double c = cosn(i, 160); r.c[i] = bf(c); r.c2[i] = bf(2 * c); } for (int i = 0; i < 64; ++i) r.e[i] = bf(cosn(i, 64)); return r; }
constexpr CT ct = mkct();
constexpr F50 mkF1() {
    F50 t{};
    for (int tile = 0; tile < 10; ++tile)
        for (int ks = 0; ks < 5; ++ks)
            for (int l = 0; l < 64; ++l)
                for (int i = 0; i < 8; ++i) {
                    int jj = 32 * ks + 16 * (i >> 2) + 4 * (l >> 4) + (i & 3), k0 = 16 * (tile >> 1) + (l & 15);
                    t.f[tile * 5 + ks].v[l * 8 + i] = !(tile & 1) ? ct.c[jj * k0 % 160] : k0 ? ct.c[(jj * k0 + 40) % 160] : ct.c[jj * 80 % 160];
                }
    return t;
}
constexpr F50 mkI1() {
    F50 t{};
    for (int ks = 0; ks < 5; ++ks)
        for (int ph = 0; ph < 10; ++ph)
            for (int l = 0; l < 64; ++l)
                for (int i = 0; i < 8; ++i) {
                    int k0 = 16 * ks + 8 * (i >> 2) + 2 * (l >> 4) + ((i >> 1) & 1), part = i & 1, j = 32 * (ph >> 1) + 2 * (l & 15) + (ph & 1);
                    t.f[ks * 10 + ph].v[l * 8 + i] = k0 ? ct.c2[(j * k0 + 40 * part) % 160] : ct.c[j * 80 * part % 160];
                }
    return t;
}
constexpr u16 e64(int i) { return ct.e[((i % 64) + 64) % 64]; }
constexpr F32 mkT() {
    F32 f{};
    for (int l = 0; l < 64; ++l)
        for (int j = 0; j < 8; ++j) {
            int m = l & 15, g = l >> 4, perm = 16 * (j >> 2) + 4 * g + (j & 3), o = l * 8 + j;
            for (int a = 0; a < 2; ++a) {
                int x = (8 * g + j) * (16 * a + m) * 2, r = 16 * a + m;
                f.f[3 * a + 0].v[o] = e64(x); f.f[3 * a + 1].v[o] = e64(x - 16); f.f[3 * a + 2].v[o] = e64(x + 16);
                x = 2 * r * perm;
                f.f[6 + 3 * a].v[o] = e64(x); f.f[7 + 3 * a].v[o] = e64(x - 16); f.f[8 + 3 * a].v[o] = e64(x + 16);
                x = r * (2 * perm + 1);
                f.f[12 + 3 * a].v[o] = e64(x); f.f[13 + 3 * a].v[o] = e64(x - 16); f.f[14 + 3 * a].v[o] = e64(x + 16);
                x = perm * (2 * r + 1);
                f.f[18 + 3 * a].v[o] = e64(x); f.f[19 + 3 * a].v[o] = e64(x - 16); f.f[20 + 3 * a].v[o] = e64(x + 16);
                for (int w = 0; w < 2; ++w) {
                    int y = (16 * w + m) * (16 * a + 4 * g + (j & 3)) * 2;
                    f.f[24 + 4 * w + 2 * a].v[o] = e64(j < 4 ? y : y + 16);
                    f.f[25 + 4 * w + 2 * a].v[o] = e64(j < 4 ? y - 16 : y);
                }
            }
        }
    return f;
}
#endif
}
#if defined(__HIP_DEVICE_COMPILE__)
static __device__ __attribute__((aligned(1024))) const tab::F50 g_F = tab::mkF1();
static __device__ __attribute__((aligned(1024))) const tab::F50 g_I = tab::mkI1();
static __device__ __attribute__((aligned(1024))) const tab::F32 g_T = tab::mkT();
#else
static __device__ const tab::F50 g_F = {};
static __device__ const tab::F50 g_I = {};
static __device__ const tab::F32 g_T = {};
#endif

constexpr int WS_HF = 0;
constexpr int WS_XF = WS_HF + 160 * 32768;
constexpr int WS_WT = WS_XF + 320 * 8192;
constexpr int WS_GT = WS_WT + 321 * 8192;
constexpr int WS_R = WS_GT + 64 * 40960;
constexpr int WS_OG = WS_R + 2048 * 1280;
constexpr int WS_GR = WS_OG + 8192;
constexpr int WS_FL = WS_GR + 10 * 16 * 1024;
constexpr int FL_S1 = 0, FL_F1 = 160, FL_W = 224, FL_F2 = 256, FL_F3 = 344;
constexpr int WS_EP = WS_FL + 512 * 128;
constexpr int WS_END = WS_EP + 256;

typedef __amdgpu_buffer_rsrc_t rsrc_t;
DI void glds16(const void* g, char* l) { __builtin_amdgcn_global_load_lds((const GAS u32*)g, (LAS u32*)l, 16, 0, 0); }
DI u32x4 ld128(rsrc_t r, int v, int s) { return __builtin_amdgcn_raw_buffer_load_b128(r, v, s, 16); }
DI u32x2 ld64(rsrc_t r, int v, int s) { return __builtin_amdgcn_raw_buffer_load_b64(r, v, s, 16); }
DI u32 ld32(rsrc_t r, int v, int s) { return __builtin_amdgcn_raw_buffer_load_b32(r, v, s, 16); }
DI void st128(u32x4 x, rsrc_t r, int v, int s) { __builtin_amdgcn_raw_buffer_store_b128(x, r, v, s, 16); }
DI void st32(u32 x, rsrc_t r, int v, int s) { __builtin_amdgcn_raw_buffer_store_b32(x, r, v, s, 16); }
DI u32 pk2(float a, float b) { f32x2 v = {a, b}; return __builtin_bit_cast(u32, __builtin_convertvector(v, bf16x2)); }
DI u32 pk2(f32x2 v) { return __builtin_bit_cast(u32, __builtin_convertvector(v, bf16x2)); }
DI u32 f2u(float f) { return __builtin_bit_cast(u32, f); }
DI float u2f(u32 u) { return __builtin_bit_cast(float, u); }
DI float bflo(u32 u) { return __builtin_bit_cast(float, u << 16); }
DI float bfhi(u32 u) { return __builtin_bit_cast(float, u & 0xffff0000u); }
DI float bf2f(u16 u) { return __builtin_bit_cast(float, (u32)u << 16); }
DI float rbf(float a) { return bflo(pk2(a, 0.f)); }
DI bf16x8 ldsv(const char* p) { return *(const LAS bf16x8*)(p); }
DI bf16x8 asfrag(u32x4 v) { return __builtin_bit_cast(bf16x8, v); }
DI bf16x8 asfrag(u32 a, u32 b, u32 c, u32 d) { u32x4 v = {a, b, c, d}; return __builtin_bit_cast(bf16x8, v); }
DI bf16x8 trfrag(const char* p0, const char* p1) {
    s16x4 lo = __builtin_amdgcn_ds_read_tr16_b64_v4i16((LAS s16x4*)p0);
    s16x4 hi = __builtin_amdgcn_ds_read_tr16_b64_v4i16((LAS s16x4*)p1);
    return __builtin_bit_cast(bf16x8, __builtin_shufflevector(lo, hi, 0, 1, 2, 3, 4, 5, 6, 7));
}
DI f32x2 lo2(f32x4 v) { return __builtin_shufflevector(v, v, 0, 1); }
DI f32x2 hi2(f32x4 v) { return __builtin_shufflevector(v, v, 2, 3); }
DI f32x4 cat(f32x2 a, f32x2 b) { return __builtin_shufflevector(a, b, 0, 1, 2, 3); }
DI f32x2 sp(float a) { return f32x2{a, a}; }
DI f32x2 fma2(f32x2 a, f32x2 b, f32x2 c) { return __builtin_elementwise_fma(a, b, c); }
#define MFMA16(a, b, c) __builtin_amdgcn_mfma_f32_16x16x32_bf16((a), (b), (c), 0, 0, 0)
#define DSR(dst, addr, off) asm volatile("ds_read_b128 %0, %1 offset:%2" : "=v"(dst) : "v"(addr), "i"(off))
#define WAIT_VM(n) asm volatile("s_waitcnt vmcnt(%0)" ::"i"(n) : "memory")
#define WAIT_LGKM0() asm volatile("s_waitcnt lgkmcnt(0)" ::: "memory")
#define BAR() do { asm volatile("" ::: "memory"); __builtin_amdgcn_s_barrier(); asm volatile("" ::: "memory"); } while (0)
#define SCHED() __builtin_amdgcn_sched_barrier(0)
#define RLX_AGENT __ATOMIC_RELAXED, __HIP_MEMORY_SCOPE_AGENT
#define STAMP(i)
DI f32x2 cmul(f32x2 a, f32x2 b) { return f32x2{a[0] * b[0] - a[1] * b[1], a[0] * b[1] + a[1] * b[0]}; }
DI f32x2 cfma(f32x2 a, f32x2 b, f32x2 c) { return f32x2{fmaf(a[0], b[0], fmaf(-a[1], b[1], c[0])), fmaf(a[0], b[1], fmaf(a[1], b[0], c[1]))}; }

constexpr int SPIN_MAX = 1 << 18;
DI u64 flagval(u32 tag) { return ((u64)tag << 32) | (tag * 22695477u + 1u); }
DI void set_flag(char* ws, int idx, u32 tag) { __hip_atomic_store((GAS u64*)(ws + WS_FL + idx * 128), flagval(tag), RLX_AGENT); }
DI void wait_flags(char* ws, int idx0, int n, u32 tag, int l) {
    GAS u64* p0 = (GAS u64*)(ws + WS_FL + (idx0 + (l < n ? l : 0)) * 128);
    GAS u64* p1 = (GAS u64*)(ws + WS_FL + (idx0 + (64 + l < n ? 64 + l : 0)) * 128);
    const u64 want = flagval(tag);
    for (int spin = 0; spin < SPIN_MAX; ++spin) {
        bool ok = __hip_atomic_load(p0, RLX_AGENT) == want;
        if (n > 64) ok &= __hip_atomic_load(p1, RLX_AGENT) == want;
        if (__all(ok)) break;
        __builtin_amdgcn_s_sleep(1);
    }
    __builtin_amdgcn_fence(__ATOMIC_ACQUIRE, "wavefront");
}

struct Args {
    const u16 *x, *wr, *wi, *lre, *lim, *bre, *bim, *cre, *cim, *ldt, *dsk, *gw;
    char* ws;
    float* out;
};

constexpr int STG = 32768;
constexpr int L_TB = 120 * 1024;
constexpr int L_PAR = 152 * 1024, L_AGG = L_PAR + 1024, L_HP = L_AGG + 4096;
#define NP1 8
#define PFD 5
#define KROT 1

DI void s_chain(const Args& A, char* lds, int xcd, int j, int wid, int l, u32 epraw) {
    char* ws = A.ws;
    const int m = 4 * (xcd >> 1) + (j & 3), ct = 5 * (xcd & 1) + (j >> 2), dt = ct;
    const rsrc_t rs = __builtin_amdgcn_make_buffer_rsrc(ws, 0, WS_END, 0x00020000);
    const int n = l & 15, g = l >> 4;
    u32 tag;
    auto issue_c = [&](int i, int hw) {
        int c2 = (i >> 1) + dt; if (c2 >= 10) c2 -= 10;
        const char* src = (const char*)((i & 1) ? A.cim : A.cre) + (size_t)(64 * dt + 8 * hw + (l >> 3)) * 1280 + c2 * 128 + (((l & 7) ^ ((4 * hw + g) & 7)) * 16);
#pragma unroll
        for (int k = 0; k < 4; ++k) glds16(src + k * 16 * 1280, lds + i * 8192 + hw * 1024 + k * 2048);
    };
    if (wid >= 4) {
        const int w = wid - 4, rl = 8 * w + (l >> 3);
        const int k0 = KROT ? (2 * (j >> 2) + 3 * (j & 3)) % 10 : 0;
        const char* pa0 = (const char*)A.x + (size_t)(128 * m + rl) * 1280 + ((l & 7) ^ w) * 16;
        const char* pa1 = (const char*)A.x + (size_t)(128 * m + 32 + rl) * 1280 + ((l & 7) ^ w ^ 4) * 16;
        const size_t bo = (size_t)(64 * ct + rl) * 1280 + ((l & 7) ^ ((4 * w + g) & 7)) * 16;
        const char *pb0 = (const char*)A.bre + bo, *pb1 = (const char*)A.bim + bo;
        auto issue = [&](int st) {
            int kk = st + k0; if (kk >= 10) kk -= 10;
            char* d = lds + (st & 3) * STG + w * 1024;
            glds16(pa0 + kk * 128, d); glds16(pb0 + kk * 128, d + 16384);
            glds16(pa1 + kk * 128, d + 4096); glds16(pb0 + 32 * 1280 + kk * 128, d + 20480);
            glds16(pa0 + 64 * 1280 + kk * 128, d + 8192); glds16(pb1 + kk * 128, d + 24576);
            glds16(pa1 + 64 * 1280 + kk * 128, d + 12288); glds16(pb1 + 32 * 1280 + kk * 128, d + 28672);
        };
        issue(0); issue(1); issue(2);
#pragma unroll
        for (int st = 0; st < 10; ++st) {
            if (st <= 7) WAIT_VM(16); else if (st == 8) WAIT_VM(8); else WAIT_VM(0);
            BAR();
            if (st + 3 < 10) issue(st + 3);
        }
        tag = __builtin_amdgcn_readfirstlane(epraw) * 1664525u + 1013904223u;
        STAMP(1);
        BAR();
        const f32x4 pv = *(const LAS f32x4*)(lds + L_PAR + l * 16);
        const f32x2 l16 = lo2(pv), l128 = hi2(pv);
        GAS u64* gr = (GAS u64*)(ws + WS_GR) + ct * 2048 + l * 2;
        if (w >= 2) {
            if (w == 2) {
                f32x2 e[8];
#pragma unroll
                for (int sg = 0; sg < 8; ++sg) e[sg] = *(const LAS f32x2*)(lds + L_AGG + sg * 512 + l * 8);
                const f32x2 l32 = cmul(l16, l16), l64 = cmul(l32, l32);
                const f32x2 t = cfma(l64, cfma(l32, cfma(l16, e[0], e[1]), cfma(l16, e[2], e[3])), cfma(l32, cfma(l16, e[4], e[5]), cfma(l16, e[6], e[7])));
                if (m < 15) {
                    __hip_atomic_store(gr + m * 128, ((u64)tag << 32) | f2u(t[0]), RLX_AGENT);
                    __hip_atomic_store(gr + m * 128 + 1, ((u64)tag << 32) | f2u(t[1]), RLX_AGENT);
                }
            } else __builtin_amdgcn_s_sleep(4);
            for (int i = 0; i < NP1; ++i) issue_c(i, w - 2);
        } else {
            const f32x2 l256 = cmul(l128, l128);
            f32x2 ev[8];
            for (int spin = 0; spin < SPIN_MAX; ++spin) {
                bool ok = true;
#pragma unroll
                for (int i = 0; i < 8; ++i) {
                    const int mp = m - 1 - w - 2 * i;
                    ev[i] = f32x2{0.f, 0.f};
                    if (mp >= 0) {
                        u64 a = __hip_atomic_load(gr + mp * 128, RLX_AGENT), b = __hip_atomic_load(gr + mp * 128 + 1, RLX_AGENT);
                        ok &= (u32)(a >> 32) == tag && (u32)(b >> 32) == tag;
                        ev[i] = f32x2{__builtin_bit_cast(float, (u32)a), __builtin_bit_cast(float, (u32)b)};
                    }
                }
                if (__all(ok)) break;
                __builtin_amdgcn_s_sleep(1);
            }
            f32x2 h = ev[7];
#pragma unroll
            for (int i = 6; i >= 0; --i) h = cfma(l256, h, ev[i]);
            if (w) h = cmul(h, l128);
            *(LAS f32x2*)(lds + L_HP + w * 512 + l * 8) = h;
            WAIT_LGKM0();
        }
        STAMP(2);
        BAR();
        WAIT_VM(0);
        BAR();
        if (w >= 2) { for (int i = NP1; i < 19; ++i) issue_c(i, w - 2); }
    } else {
        const int wm = wid >> 1, wn = wid & 1, seg = 4 * wm + g;
        f32x2 lam[2], kap[2], l8[2], l16[2], lsg[2], q[2][8];
#pragma unroll
        for (int a = 0; a < 2; ++a) {
            const int p = 64 * ct + 32 * wn + 16 * a + n;
            const float ar = bf2f(A.lre[p]), ai = bf2f(A.lim[p]), dl = rbf(expf(bf2f(A.ldt[p])));
            float ex = expf(dl * ar), sn, cs;
            sincosf(dl * ai, &sn, &cs);
            lam[a] = f32x2{ex * cs, ex * sn};
            const float den = 2.f / (ar * ar + ai * ai), nr = lam[a][0] - 1.f;
            kap[a] = f32x2{(nr * ar + lam[a][1] * ai) * den, (lam[a][1] * ar - nr * ai) * den};
            f32x2 pw = lam[a];
#pragma unroll
            for (int i = 0; i < 8; ++i) { q[a][i] = cmul(kap[a], pw); if (i < 7) pw = cmul(pw, lam[a]); }
            l8[a] = pw; l16[a] = cmul(pw, pw);
            const f32x2 l32 = cmul(l16[a], l16[a]), l64 = cmul(l32, l32), one = {1.f, 0.f};
            lsg[a] = cmul(cmul((g & 1) ? l16[a] : one, (g & 2) ? l32 : one), wm ? l64 : one);
            if (seg == 0) *(LAS f32x4*)(lds + L_PAR + (32 * wn + 16 * a + n) * 16) = cat(l16[a], cmul(l64, l64));
        }
        tag = __builtin_amdgcn_readfirstlane(epraw) * 1664525u + 1013904223u;
        f32x4 acc[4][4];
#pragma unroll
        for (int i = 0; i < 16; ++i) acc[i >> 2][i & 3] = f32x4{0.f, 0.f, 0.f, 0.f};
        {
            const int rowA = 64 * wm + 16 * (n >> 2) + 8 * (n & 1) + ((n >> 1) & 1), swA = 2 * (n >> 2) + (n & 1);
            int aa[2][2], ba[2][2];
#pragma unroll
            for (int ks = 0; ks < 2; ++ks) {
                aa[0][ks] = rowA * 128 + (((4 * ks + g) ^ swA) * 16); ba[0][ks] = 16384 + (32 * wn + n) * 128 + (((4 * ks + g) ^ (n >> 1)) * 16);
                aa[1][ks] = aa[0][ks] + 65536; ba[1][ks] = ba[0][ks] + 65536;
            }
            bf16x8 fa[2][4], fb[2][4];
#pragma unroll
            for (int st = 0; st < 10; ++st) {
                WAIT_LGKM0();
                BAR();
                SCHED();
#pragma unroll
                for (int ks = 0; ks < 2; ++ks) {
#pragma unroll
                    for (int i = 0; i < 16; ++i) {
                        if (st + ks > 0) acc[i >> 2][i & 3] = MFMA16(fa[ks ^ 1][i >> 2], fb[ks ^ 1][i & 3], acc[i >> 2][i & 3]);
                        SCHED();
                        const int rd = i < 4 ? i : (i < 12 && !(i & 1)) ? 2 + i / 2 : -1;
                        if (rd >= 0 && rd < 4) DSR(fb[ks][rd], ba[(st >> 1) & 1][ks], (st & 1) * STG + (rd >> 1) * 2048 + (rd & 1) * 8192);
                        if (rd >= 4) DSR(fa[ks][rd - 4], aa[(st >> 1) & 1][ks], (st & 1) * STG + (rd - 4) * 256);
                        SCHED();
                    }
                    if (ks == 0) { WAIT_LGKM0(); SCHED(); }
                }
            }
            WAIT_LGKM0();
            SCHED();
#pragma unroll
            for (int i = 0; i < 16; ++i) acc[i >> 2][i & 3] = MFMA16(fa[1][i >> 2], fb[1][i & 3], acc[i >> 2][i & 3]);
        }
        STAMP(1);
        f32x2 ur[2][8], ui[2][8], ea[2];
#pragma unroll
        for (int a = 0; a < 2; ++a) {
#pragma unroll
            for (int t = 0; t < 8; ++t) {
                ur[a][t] = (t & 1) ? hi2(acc[t >> 1][2 * a]) : lo2(acc[t >> 1][2 * a]);
                ui[a][t] = (t & 1) ? hi2(acc[t >> 1][2 * a + 1]) : lo2(acc[t >> 1][2 * a + 1]);
            }
        }
#pragma unroll
        for (int t = 1; t < 8; ++t)
#pragma unroll
            for (int a = 0; a < 2; ++a) {
                ur[a][t] = fma2(sp(lam[a][0]), ur[a][t - 1], fma2(sp(-lam[a][1]), ui[a][t - 1], ur[a][t]));
                ui[a][t] = fma2(sp(lam[a][0]), ui[a][t - 1], fma2(sp(lam[a][1]), ur[a][t - 1], ui[a][t]));
            }
        char* agp = lds + L_AGG + (32 * wn + n) * 8;
#pragma unroll
        for (int a = 0; a < 2; ++a) {
            ea[a] = f32x2{ur[a][7][0], ui[a][7][0]};
            *(LAS f32x2*)(agp + seg * 512 + a * 128) = cfma(l8[a], ea[a], f32x2{ur[a][7][1], ui[a][7][1]});
        }
        WAIT_LGKM0();
        BAR();
        f32x2 pre[2];
#pragma unroll
        for (int a = 0; a < 2; ++a) {
            pre[a] = f32x2{0.f, 0.f};
#pragma unroll
            for (int sg = 0; sg < 7; ++sg) {
                const f32x2 nx = cfma(l16[a], pre[a], *(const LAS f32x2*)(agp + sg * 512 + a * 128));
                if (sg < seg) pre[a] = nx;
            }
#pragma unroll
            for (int t = 0; t < 8; ++t) {
                const f32x2 r = ur[a][t], i = ui[a][t];
                ur[a][t] = fma2(sp(kap[a][0]), r, sp(-kap[a][1]) * i);
                ui[a][t] = fma2(sp(-kap[a][0]), i, sp(-kap[a][1]) * r);
            }
        }
        STAMP(2);
        BAR();
        STAMP(3);
        char* tb = lds + L_TB + wid * 8192;
        {
            const int sw = (n & 3) | ((((n >> 3) ^ (n >> 2)) & 1) << 2);
#pragma unroll
            for (int a = 0; a < 2; ++a) {
                const char* hp = lds + L_HP + (32 * wn + 16 * a + n) * 8;
                const f32x2 hin = *(const LAS f32x2*)(hp) + *(const LAS f32x2*)(hp + 512);
                const f32x2 ca = cfma(lsg[a], hin, pre[a]), cb = cfma(l8[a], ca, ea[a]);
                const f32x2 cr = {ca[0], cb[0]}, ci = {ca[1], cb[1]};
                u32 pr[8], pi[8];
#pragma unroll
                for (int t = 0; t < 8; ++t) {
                    pr[t] = pk2(fma2(sp(q[a][t][0]), cr, fma2(sp(-q[a][t][1]), ci, ur[a][t])));
                    pi[t] = pk2(fma2(sp(-q[a][t][0]), ci, fma2(sp(-q[a][t][1]), cr, ui[a][t])));
                }
#pragma unroll
                for (int h = 0; h < 2; ++h) {
                    char* wp = tb + (16 * a + n) * 128 + (((2 * g + h) ^ sw) * 16);
                    *(LAS u32x4*)(wp) = u32x4{pr[4 * h], pr[4 * h + 1], pr[4 * h + 2], pr[4 * h + 3]};
                    *(LAS u32x4*)(wp + 4096) = u32x4{pi[4 * h], pi[4 * h + 1], pi[4 * h + 2], pi[4 * h + 3]};
                }
            }
            const int r = n >> 2, so = WS_HF + (m * 10 + ct) * 32768 + wn * 16384 + wm * 4096;
            const int s0 = r | ((g & 1) << 2), c0 = (n >> 1) & 1;
            const char* rp = tb + (8 * g + r) * 128 + (n & 1) * 8;
#pragma unroll
            for (int part = 0; part < 2; ++part)
#pragma unroll
                for (int tt = 0; tt < 4; ++tt) {
                    const bf16x8 f = trfrag(rp + part * 4096 + (((2 * tt + c0) ^ s0) * 16), rp + part * 4096 + 512 + (((2 * tt + c0) ^ s0 ^ 4) * 16));
                    st128(__builtin_bit_cast(u32x4, f), rs, l * 16 + tt * 1024, so + part * 8192);
                }
        }
        STAMP(4);
        WAIT_VM(0);
        BAR();
        if (wid == 0 && l == 0) set_flag(ws, FL_S1 + m * 10 + ct, tag);
    }
    STAMP(5);
    const int tp = wid & 3, kh = wid >> 2;
    bf16x8 cl[4];
    {
        int c2 = 9 + dt; if (c2 >= 10) c2 -= 10;
        const char* src = (const char*)A.cim + (size_t)(64 * dt + n) * 1280 + c2 * 128 + kh * 64 + g * 16;
#pragma unroll
        for (int d = 0; d < 4; ++d) cl[d] = *(const GAS bf16x8*)(src + d * 16 * 1280);
    }
    if (wid == 4) wait_flags(ws, FL_S1 + m * 10, 10, tag, l);
    BAR();
    STAMP(6);
    f32x4 acc[4][2];
#pragma unroll
    for (int i = 0; i < 8; ++i) acc[i >> 1][i & 1] = f32x4{0.f, 0.f, 0.f, 0.f};
    const int tile = 2 * tp + kh, token = 128 * m + 16 * tile + 8 * (n & 1) + (n >> 1);
    const u64 want = flagval(tag);
    GAS u64* f3 = (GAS u64*)(ws + WS_FL + (FL_F3 + 4 * m + 2 * (tp >> 1) + (l & 1)) * 128);
    u64 fv = 0;
    bool have = false;
    u32x2 rv[4]; u32 og = 0;
    const int ro = WS_R + token * 1280 + dt * 128 + g * 8;
    {
        const int hv0 = WS_HF + m * 327680 + kh * 16384 + tile * 1024 + l * 16, hv1 = hv0 + (kh ? -1024 : 1024);
        bf16x8 hf[PFD + 1][2], cf[2][4];
        auto ldh = [&](int i) {
            int c2 = (i >> 1) + dt; if (c2 >= 10) c2 -= 10;
            const int so = c2 * 32768 + (i & 1) * 8192;
            hf[i % (PFD + 1)][0] = asfrag(ld128(rs, hv0, so)); hf[i % (PFD + 1)][1] = asfrag(ld128(rs, hv1, so));
        };
#pragma unroll
        for (int i = 0; i < PFD; ++i) ldh(i);
        int ca[3];
        ca[0] = n * 128 + (((4 * kh + g) ^ (n >> 1)) * 16); ca[1] = ca[0] + 65536; ca[2] = ca[0] + 131072;
#pragma unroll
        for (int d = 0; d < 4; ++d) DSR(cf[0][d], ca[0], d * 2048);
#pragma unroll
        for (int i = 0; i < 20; ++i) {
            if (i == NP1 - 1) BAR();
            if (i + PFD < 20) ldh(i + PFD);
            if (i == 9) fv = __hip_atomic_load(f3, RLX_AGENT);
            if (i == 14) {
                have = __all(fv == want);
                __builtin_amdgcn_fence(__ATOMIC_ACQUIRE, "wavefront");
#pragma unroll
                for (int d = 0; d < 4; ++d) rv[d] = ld64(rs, (have ? ro : WS_END) + d * 32, 0);
                og = ld32(rs, have ? WS_OG + token * 4 : WS_END, 0);
            }
            WAIT_LGKM0();
            SCHED();
#pragma unroll
            for (int k = 0; k < 8; ++k) {
                const int d = k >> 1, t = k & 1;
                acc[d][t] = MFMA16(i == 19 ? cl[d] : cf[i & 1][d], hf[i % (PFD + 1)][t], acc[d][t]);
                SCHED();
                if (k < 4 && i + 1 < 19) DSR(cf[(i + 1) & 1][k], ca[(i + 1) >> 3], ((i + 1) & 7) * 8192 + k * 2048);
                SCHED();
            }
        }
    }
    STAMP(7);
#pragma unroll
    for (int d = 0; d < 4; ++d) *(LAS f32x4*)(lds + wid * 4096 + d * 1024 + l * 16) = acc[d][1];
    if (!have) {
        for (int spin = 0; spin < SPIN_MAX; ++spin) {
            if (__all(__hip_atomic_load(f3, RLX_AGENT) == want)) break;
            __builtin_amdgcn_s_sleep(1);
        }
        __builtin_amdgcn_fence(__ATOMIC_ACQUIRE, "wavefront");
#pragma unroll
        for (int d = 0; d < 4; ++d) rv[d] = ld64(rs, ro + d * 32, 0);
        og = ld32(rs, WS_OG + token * 4, 0);
    }
    WAIT_LGKM0();
    BAR();
    STAMP(8);
    float* op = A.out + (size_t)token * 640 + 64 * dt + 4 * g;
    const float ogf = __builtin_bit_cast(float, og);
#pragma unroll
    for (int d = 0; d < 4; ++d) {
        const f32x4 y = acc[d][0] + *(const LAS f32x4*)(lds + (wid ^ 4) * 4096 + d * 1024 + l * 16);
        const f32x4 o = {fmaf(ogf, y[0], bflo(rv[d][0])), fmaf(ogf, y[1], bfhi(rv[d][0])), fmaf(ogf, y[2], bflo(rv[d][1])), fmaf(ogf, y[3], bfhi(rv[d][1]))};
        *(GAS f32x4*)(op + 16 * d) = o;
    }
    if (m == 15 && dt == 0 && wid == 0 && l == 0) __hip_atomic_store((GAS u32*)(ws + WS_EP), tag, RLX_AGENT);
    STAMP(9);
}

constexpr int L_XR = 40960, L_X1 = 81920, L_GS = 147456, L_I2 = 147456 + 1024;

DI void f_chain(const Args& A, char* lds, int f, int wid, int l, u32 epraw) {
    char* ws = A.ws;
    const rsrc_t rs = __builtin_amdgcn_make_buffer_rsrc(ws, 0, WS_END, 0x00020000);
    const int n = l & 15, g = l >> 4;
    const f32x4 z4 = {0.f, 0.f, 0.f, 0.f};
    u32 tag;
    if (f < 64) {
        const int t = f >> 4, gq = (f >> 2) & 3, quad = f & 3;
#pragma unroll
        for (int it = 0; it < 5; ++it) {
            const int L = (it * 8 + wid) * 64 + l, row = L / 80, ch = L - row * 80;
            const int tok = 64 * (8 * gq + (row >> 2)) + 32 * (t & 1) + 2 * (4 * quad + (row & 3)) + (t >> 1);
            glds16((const char*)A.x + (size_t)tok * 1280 + ((ch ^ ((row & 3) << 2)) * 16), lds + (it * 8 + wid) * 1024);
        }
        const int tile0 = wid < 4 ? 2 * wid : wid < 6 ? 4 + wid : 0, two = wid < 4;
        bf16x8 fb[2][5];
#pragma unroll
        for (int k = 0; k < 10; ++k) fb[k / 5][k % 5] = *(const GAS bf16x8*)((const char*)&g_F + ((tile0 + (two ? k / 5 : 0)) * 5 + k % 5) * 1024 + l * 16);
        tag = __builtin_amdgcn_readfirstlane(epraw) * 1664525u + 1013904223u;
        WAIT_VM(0);
        BAR();
        STAMP(1);
        if (wid < 6) {
            f32x4 acc[2][8];
            const int b = n & 3;
            const char* a0 = lds + b * 1280 + (((g >> 1) ^ b) * 64) + (4 * (g & 1) + (n >> 2)) * 8;
            const char* a1 = lds + b * 1280 + (((2 + (g >> 1)) ^ b) * 64) + (4 * (g & 1) + (n >> 2)) * 8;
            if (two) {
#pragma unroll
                for (int ks = 0; ks < 5; ++ks)
#pragma unroll
                    for (int jx = 0; jx < 8; ++jx) {
                        const bf16x8 fa = trfrag(a0 + jx * 5120 + ks * 256, a1 + jx * 5120 + ks * 256);
                        acc[0][jx] = MFMA16(fa, fb[0][ks], ks ? acc[0][jx] : z4);
                        acc[1][jx] = MFMA16(fa, fb[1][ks], ks ? acc[1][jx] : z4);
                    }
            } else {
#pragma unroll
                for (int ks = 0; ks < 5; ++ks)
#pragma unroll
                    for (int jx = 0; jx < 8; ++jx) acc[0][jx] = MFMA16(trfrag(a0 + jx * 5120 + ks * 256, a1 + jx * 5120 + ks * 256), fb[0][ks], ks ? acc[0][jx] : z4);
            }
            const int vo = n * 32768 + g * 16, so = WS_XF + (tile0 >> 1) * 16 * 32768 + t * 2048 + gq * 512 + quad * 128 + (tile0 & 1) * 64;
#pragma unroll
            for (int p = 0; p < 2; ++p)
                if (p == 0 || two) {
#pragma unroll
                    for (int c = 0; c < 4; ++c) {
                        const u32x4 v = {pk2(acc[p][0][c], acc[p][1][c]), pk2(acc[p][2][c], acc[p][3][c]), pk2(acc[p][4][c], acc[p][5][c]), pk2(acc[p][6][c], acc[p][7][c])};
                        st128(v, rs, vo + p * 64, so + c * 8192);
                    }
                }
        }
        STAMP(2);
        WAIT_VM(0);
        BAR();
        if (wid == 0 && l == 0) set_flag(ws, FL_F1 + f, tag);
#pragma unroll
        for (int it = 0; it < 5; ++it) {
            const int L = (it * 8 + wid) * 64 + l, row = L / 80, ch = L - row * 80;
            const int tok = 64 * (f >> 1) + 2 * (16 * ((row >> 2) & 1) + 4 * (row >> 3) + (row & 3)) + (f & 1);
            glds16((const char*)A.x + (size_t)tok * 1280 + ch * 16, lds + L_XR + (it * 8 + wid) * 1024);
        }
    } else {
        const int r = f - 64;
        for (int i = wid; i < 82; i += 8) {
            const int arr = i >= 41, k = i - 41 * arr;
            int o = k * 1024 + l * 16;
            if (o > 41088 - 16) o = 41088 - 16;
            glds16((const char*)(arr ? A.wi : A.wr) + (size_t)r * 41088 + o, lds + L_XR + i * 1024);
        }
        tag = __builtin_amdgcn_readfirstlane(epraw) * 1664525u + 1013904223u;
        WAIT_VM(0);
        BAR();
        STAMP(1);
        const char* p = lds + L_XR + n * 4 * 642;
        for (int c = wid * 4 + g; c < 321; c += 32) {
            u32x4 v;
#pragma unroll
            for (int k = 0; k < 4; ++k) v[k] = (u32)*(const LAS u16*)(p + c * 2 + k * 642) | ((u32)*(const LAS u16*)(p + c * 2 + k * 642 + 41 * 1024) << 16);
            st128(v, rs, c * 8192 + n * 16, WS_WT + r * 256);
        }
        STAMP(2);
        WAIT_VM(0);
        BAR();
        if (wid == 0 && l == 0) set_flag(ws, FL_W + r, tag);
        if (f > 80) return;
    }
    STAMP(3);
#pragma unroll
    for (int i = 0; i < 4; ++i) glds16((const char*)&g_T + (i * 8 + wid) * 1024 + l * 16, lds + (i * 8 + wid) * 1024);
    const int c = wid >> 1, a = wid & 1, ka = 16 * a + n;
    f32x4 tc[4], ts[4], uc[4], us[4];
#pragma unroll
    for (int i = 0; i < 16; ++i) {
        const int e = i >> 3, u = (i >> 2) & 1, cc = i & 3;
        const float f1 = (float)((2 * (16 * u + 4 * g + cc) + e) * ka) * (1.f / 2048.f);
        const float f2 = (float)((2 * (16 * u + n) + e) * (16 * a + 4 * g + cc)) * (1.f / 2048.f);
        tc[i >> 2][cc] = __builtin_amdgcn_cosf(f1); ts[i >> 2][cc] = __builtin_amdgcn_sinf(f1);
        uc[i >> 2][cc] = __builtin_amdgcn_cosf(f2); us[i >> 2][cc] = __builtin_amdgcn_sinf(f2);
    }
    if (wid == 0) wait_flags(ws, FL_W, 32, tag, l);
    WAIT_VM(0);
    BAR();
    STAMP(4);
    f32x2 vr[2][7], vi[2][7];
    {
        u32 wv[2][2][4];
#pragma unroll
        for (int hi = 0; hi < 2; ++hi)
#pragma unroll
            for (int x = 0; x < 2; ++x) {
                const int s = ka + 32 * (16 * (c >> 1) + 4 * g + 2 * (c & 1) + x + 32 * hi);
                const int sm = (2048 - s) & 2047;
                wv[hi][x][0] = ld32(rs, s * 4, WS_WT + f * 8192); wv[hi][x][1] = ld32(rs, s * 4, WS_WT + (160 + f) * 8192);
                wv[hi][x][2] = ld32(rs, sm * 4, WS_WT + (320 - f) * 8192); wv[hi][x][3] = ld32(rs, sm * 4, WS_WT + (160 - f) * 8192);
            }
        if (f < 64) {
            const char* xp = lds + L_XR + (4 * wid + g) * 1280 + n * 16;
            float sum = 0.f;
#pragma unroll
            for (int i = 0; i < 5; ++i) {
                const u32x4 xv = *(const LAS u32x4*)(xp + i * 256), gv = *(const GAS u32x4*)((const char*)A.gw + i * 256 + n * 16);
#pragma unroll
                for (int k = 0; k < 4; ++k) { const u32 pp = pk2(bflo(xv[k]) * bflo(gv[k]), bfhi(xv[k]) * bfhi(gv[k])); sum += bflo(pp) + bfhi(pp); }
            }
            sum += __shfl_xor(sum, 1); sum += __shfl_xor(sum, 2); sum += __shfl_xor(sum, 4); sum += __shfl_xor(sum, 8);
            const float gt = rbf(1.f / (1.f + expf(-rbf(sum))));
            if (n == 0) *(LAS f32x2*)(lds + L_GS + (4 * wid + g) * 8) = f32x2{gt, rbf(1.f - gt)};
        }
        const float sc = 1.f / (2048.f * 640.f);
        float cs[4], sn[4];
#pragma unroll
        for (int d = 1; d < 4; ++d) { const float fr = (float)(d * f) * (1.f / 640.f); cs[d] = __builtin_amdgcn_cosf(fr) * sc; sn[d] = __builtin_amdgcn_sinf(fr) * sc; }
#pragma unroll
        for (int hi = 0; hi < 2; ++hi) {
            f32x2 wr[4], wi[4];
#pragma unroll
            for (int k = 0; k < 4; ++k) { wr[k] = f32x2{bflo(wv[hi][0][k]), bflo(wv[hi][1][k])}; wi[k] = f32x2{bfhi(wv[hi][0][k]), bfhi(wv[hi][1][k])}; }
            wi[2] = -wi[2]; wi[3] = -wi[3];
            const f32x2 ar = wr[0] + wr[2], ai = wi[0] + wi[2], br = wr[0] - wr[2], bi = wi[0] - wi[2];
            const f32x2 cr = wr[1] + wr[3], ci = wi[1] + wi[3], dr = wr[1] - wr[3], di = wi[1] - wi[3];
            const f32x2 Vr[4] = {ar + cr, br - di, ar - cr, br + di}, Vi[4] = {ai + ci, bi + dr, ai - ci, bi - dr};
            vr[hi][3] = Vr[0] * sp(sc); vi[hi][3] = Vi[0] * sp(sc);
#pragma unroll
            for (int d = 1; d < 4; ++d) {
                vr[hi][3 + d] = Vr[d] * sp(cs[d]) - Vi[d] * sp(sn[d]); vi[hi][3 + d] = Vi[d] * sp(cs[d]) + Vr[d] * sp(sn[d]);
                vr[hi][3 - d] = Vr[4 - d] * sp(cs[d]) + Vi[4 - d] * sp(sn[d]); vi[hi][3 - d] = Vi[4 - d] * sp(cs[d]) - Vr[4 - d] * sp(sn[d]);
            }
        }
    }
    STAMP(5);
    if (wid == 0) wait_flags(ws, FL_F1, 64, tag, l);
    WAIT_LGKM0();
    BAR();
    STAMP(6);
    const bool realonly = f == 0 || f == 80;
    const int kslot = f == 80 ? 0 : f, p0 = f == 80 ? 64 : 0;
    {
        bf16x8 xr[4], xi[4];
        const int xo = g * 512 + (n >> 2) * 128 + (n & 3) * 16, xs = WS_XF + (kslot * 4 + c) * 8192;
#pragma unroll
        for (int t = 0; t < 4; ++t) { xr[t] = asfrag(ld128(rs, xo + p0 + (t & 1) * 2048, xs + (t >> 1) * 4096)); xi[t] = asfrag(ld128(rs, realonly ? WS_END : xo + 64 + (t & 1) * 2048, xs + (t >> 1) * 4096)); }
        const char* tp = lds + l * 16;
        u32 pr[2][4], pi[2][4];
        {
            const bf16x8 C = ldsv(tp + (3 * a) * 1024), S = ldsv(tp + (3 * a + 1) * 1024), N = ldsv(tp + (3 * a + 2) * 1024);
#pragma unroll
            for (int t = 0; t < 4; ++t) {
                const f32x4 yr = MFMA16(xi[t], S, MFMA16(xr[t], C, z4)), yi = MFMA16(xr[t], N, MFMA16(xi[t], C, z4));
                const int e = t >> 1, u = t & 1;
                pr[e][2 * u] = pk2(fma2(lo2(yr), lo2(tc[t]), lo2(yi) * lo2(ts[t]))); pr[e][2 * u + 1] = pk2(fma2(hi2(yr), hi2(tc[t]), hi2(yi) * hi2(ts[t])));
                pi[e][2 * u] = pk2(fma2(lo2(yi), lo2(tc[t]), -lo2(yr) * lo2(ts[t]))); pi[e][2 * u + 1] = pk2(fma2(hi2(yi), hi2(tc[t]), -hi2(yr) * hi2(ts[t])));
            }
        }
        STAMP(7);
        bf16x8 EC[2], ES[2], EN[2];
        {
            const bf16x8 t0r = asfrag(pr[0][0], pr[0][1], pr[0][2], pr[0][3]), t0i = asfrag(pi[0][0], pi[0][1], pi[0][2], pi[0][3]);
            const bf16x8 t1r = asfrag(pr[1][0], pr[1][1], pr[1][2], pr[1][3]), t1i = asfrag(pi[1][0], pi[1][1], pi[1][2], pi[1][3]);
#pragma unroll
            for (int v = 0; v < 2; ++v) {
                EC[v] = ldsv(tp + (6 + 3 * v) * 1024); ES[v] = ldsv(tp + (7 + 3 * v) * 1024); EN[v] = ldsv(tp + (8 + 3 * v) * 1024);
                const bf16x8 OC = ldsv(tp + (12 + 3 * v) * 1024), OS = ldsv(tp + (13 + 3 * v) * 1024), ON = ldsv(tp + (14 + 3 * v) * 1024);
                const f32x4 er = MFMA16(ES[v], t0i, MFMA16(EC[v], t0r, z4)), ei = MFMA16(EN[v], t0r, MFMA16(EC[v], t0i, z4));
                const f32x4 dr = MFMA16(OS, t1i, MFMA16(OC, t1r, z4)), di = MFMA16(ON, t1r, MFMA16(OC, t1i, z4));
                char* o = lds + L_X1 + (4 * v + a) * 8192 + c * 2048 + l * 16;
                *(LAS f32x4*)(o) = cat(lo2(er), lo2(ei)); *(LAS f32x4*)(o + 1024) = cat(lo2(dr), lo2(di));
                *(LAS f32x4*)(o + 16384) = cat(hi2(er), hi2(ei)); *(LAS f32x4*)(o + 17408) = cat(hi2(dr), hi2(di));
            }
        }
        WAIT_LGKM0();
        BAR();
        STAMP(8);
        char* my = lds + L_X1 + wid * 8192 + l * 16;
        {
            f32x2 lr[4], li[4], hr[4], hi_[4];
#pragma unroll
            for (int k = 0; k < 4; ++k) {
                const f32x4 e = *(const LAS f32x4*)(my + k * 2048), d = *(const LAS f32x4*)(my + k * 2048 + 1024);
                lr[k] = lo2(e) + lo2(d); li[k] = hi2(e) + hi2(d); hr[k] = lo2(e) - lo2(d); hi_[k] = hi2(e) - hi2(d);
            }
#pragma unroll
            for (int o = 0; o < 4; ++o) {
                f32x2 gr0 = vr[0][3 + o] * lr[0], gi0 = vr[0][3 + o] * li[0], gr1 = vr[1][3 + o] * hr[0], gi1 = vr[1][3 + o] * hi_[0];
                gr0 = fma2(-vi[0][3 + o], li[0], gr0); gi0 = fma2(vi[0][3 + o], lr[0], gi0);
                gr1 = fma2(-vi[1][3 + o], hi_[0], gr1); gi1 = fma2(vi[1][3 + o], hr[0], gi1);
#pragma unroll
                for (int k = 1; k < 4; ++k) {
                    const int d = 3 + o - k;
                    gr0 = fma2(vr[0][d], lr[k], gr0); gi0 = fma2(vr[0][d], li[k], gi0); gr1 = fma2(vr[1][d], hr[k], gr1); gi1 = fma2(vr[1][d], hi_[k], gi1);
                    gr0 = fma2(-vi[0][d], li[k], gr0); gi0 = fma2(vi[0][d], lr[k], gi0); gr1 = fma2(-vi[1][d], hi_[k], gr1); gi1 = fma2(vi[1][d], hr[k], gi1);
                }
                *(LAS u32x4*)(my + o * 1024) = u32x4{pk2(gr0 + gr1), pk2(gi0 + gi1), pk2(gr0 - gr1), pk2(gi0 - gi1)};
            }
        }
        WAIT_LGKM0();
        BAR();
        STAMP(9);
        u32x4 vf[2][2];
        {
            u32x4 qv[4];
#pragma unroll
            for (int r = 0; r < 4; ++r) qv[r] = *(const LAS u32x4*)(lds + L_X1 + (2 * r + a) * 8192 + c * 1024 + l * 16);
#pragma unroll
            for (int e = 0; e < 2; ++e) {
                const bf16x8 ar = asfrag(qv[0][2 * e], qv[1][2 * e], qv[2][2 * e], qv[3][2 * e]), ai = asfrag(qv[0][2 * e + 1], qv[1][2 * e + 1], qv[2][2 * e + 1], qv[3][2 * e + 1]);
#pragma unroll
                for (int u = 0; u < 2; ++u) {
                    const bf16x8 C = e ? ldsv(tp + (18 + 3 * u) * 1024) : EC[u], S = e ? ldsv(tp + (19 + 3 * u) * 1024) : ES[u], N = e ? ldsv(tp + (20 + 3 * u) * 1024) : EN[u];
                    const f32x4 yr = MFMA16(ai, N, MFMA16(ar, C, z4)), yi = MFMA16(ar, S, MFMA16(ai, C, z4));
                    const int t = 2 * e + u;
                    vf[e][u] = u32x4{pk2(fma2(lo2(yr), lo2(uc[t]), -lo2(yi) * lo2(us[t]))), pk2(fma2(hi2(yr), hi2(uc[t]), -hi2(yi) * hi2(us[t]))),
                                     pk2(fma2(lo2(yi), lo2(uc[t]), lo2(yr) * lo2(us[t]))), pk2(fma2(hi2(yi), hi2(uc[t]), hi2(yr) * hi2(us[t])))};
                }
            }
        }
        const u32 am = a ? 0xffffffffu : 0u;
#pragma unroll
        for (int u = 0; u < 2; ++u) *(LAS u32x4*)(my + 4096 + u * 1024) = (vf[0][u] & am) | (vf[1][u] & ~am);
        WAIT_LGKM0();
        BAR();
        STAMP(10);
        bf16x8 own[2], oth[2];
#pragma unroll
        for (int u = 0; u < 2; ++u) { own[u] = asfrag((vf[1][u] & am) | (vf[0][u] & ~am)); oth[u] = ldsv(lds + L_X1 + (wid ^ 1) * 8192 + 4096 + u * 1024 + l * 16); }
        const int go = n * 81920 + g * 16, gs = WS_GT + a * 40960 + kslot * 512 + c * 128 + p0;
#pragma unroll
        for (int w = 0; w < 2; ++w)
#pragma unroll
            for (int part = 0; part < 2; ++part) {
                const bf16x8 Bo = ldsv(tp + (24 + 4 * w + 2 * a + part) * 1024), Bp = ldsv(tp + (24 + 4 * w + 2 * (a ^ 1) + part) * 1024);
                const f32x4 o0 = MFMA16(oth[0], Bp, MFMA16(own[0], Bo, z4)), o1 = MFMA16(oth[1], Bp, MFMA16(own[1], Bo, z4));
                const u32x4 v = {pk2(lo2(o0)), pk2(hi2(o0)), pk2(lo2(o1)), pk2(hi2(o1))};
                if (!(realonly && part)) st128(v, rs, go + part * 64, gs + w * 16 * 81920);
            }
    }
    STAMP(11);
    WAIT_VM(0);
    BAR();
    if (wid == 0 && l == 0) set_flag(ws, FL_F2 + f, tag);
    if (f >= 64) return;
    for (int i = wid; i < 50; i += 8) glds16((const char*)&g_I + i * 1024 + l * 16, lds + (i < 40 ? i * 1024 : L_I2 + (i - 40) * 1024));
    const int mp = wid & 3, three = wid < 4, pair0 = three ? 0 : 3;
    f32x2 tv[2][3][2][2];
    float gg[2][2];
#pragma unroll
    for (int mt = 0; mt < 2; ++mt)
#pragma unroll
        for (int y = 0; y < 2; ++y) {
            const int pos = 8 * mp + 4 * mt + 2 * y + (g & 1);
            const f32x2 gv = *(const LAS f32x2*)(lds + L_GS + pos * 8);
            gg[mt][y] = gv[0];
#pragma unroll
            for (int pp = 0; pp < 3; ++pp)
#pragma unroll
                for (int h = 0; h < 2; ++h) {
                    const int d = 128 * (pair0 + (three || pp < 2 ? pp : 0)) + 8 * n + 4 * h + 2 * (g >> 1);
                    const u32 xv = *(const LAS u32*)(lds + L_XR + pos * 1280 + d * 2), dv = *(const GAS u32*)(A.dsk + d);
                    const u32 xd = pk2(bflo(xv) * bflo(dv), bfhi(xv) * bfhi(dv));
                    tv[mt][pp][h][y] = f32x2{gv[1] * bflo(xd), gv[1] * bfhi(xd)};
                }
        }
    STAMP(12);
    if (wid == 0) wait_flags(ws, FL_F2, 81, tag, l);
    WAIT_VM(0);
    BAR();
    STAMP(13);
    {
        u32x4 gl[5];
#pragma unroll
        for (int it = 0; it < 5; ++it) gl[it] = ld128(rs, (wid * 64 + l) * 16, WS_GT + f * 40960 + it * 8192);
        const int L = wid * 64 + l;
#pragma unroll
        for (int it = 0; it < 5; ++it) *(LAS u32x4*)(lds + L_X1 + it * 8192 + ((L ^ ((L >> 5) & 3)) * 16)) = gl[it];
    }
    WAIT_LGKM0();
    BAR();
    STAMP(14);
    {
        f32x4 acc[2][6];
        const int r = n >> 2, kq = (2 * g + (r >> 1)) & 3;
        const char* ap = lds + L_X1 + (2 * g + (r >> 1)) * 512 + (n & 3) * 128 + (r & 1) * 64 + ((mp ^ kq) * 16);
        const char* bp = lds + pair0 * 2048 + l * 16;
#pragma unroll
        for (int ks = 0; ks < 5; ++ks) {
            const bf16x8 a0 = trfrag(ap + ks * 8192, ap + ks * 8192 + 4096), a1 = trfrag(ap + ks * 8192 + 8, ap + ks * 8192 + 4104);
#pragma unroll
            for (int k = 0; k < 6; ++k)
                if (k < 4 || three) {
                    const bf16x8 b = ldsv(bp + (ks < 4 ? ks * 10240 : L_I2) + k * 1024);
                    acc[0][k] = MFMA16(a0, b, ks ? acc[0][k] : z4); acc[1][k] = MFMA16(a1, b, ks ? acc[1][k] : z4);
                }
        }
#pragma unroll
        for (int mt = 0; mt < 2; ++mt) {
            const int pos = 8 * mp + 4 * mt + g, tok = 64 * (f >> 1) + 2 * (16 * mt + 4 * mp + g) + (f & 1);
            (void)pos;
#pragma unroll
            for (int pp = 0; pp < 3; ++pp)
                if (pp < 2 || three) {
                    u32x4 v;
#pragma unroll
                    for (int h = 0; h < 2; ++h) {
                        const f32x4 x4 = acc[mt][2 * pp + h];
                        const auto s0 = __builtin_amdgcn_permlane16_swap(f2u(x4[0]), f2u(x4[1]), false, false);
                        const auto s1 = __builtin_amdgcn_permlane16_swap(f2u(x4[2]), f2u(x4[3]), false, false);
                        const u32 q0 = pk2(fma2(sp(gg[mt][0]), f32x2{u2f(s0[0]), u2f(s0[1])}, tv[mt][pp][h][0]));
                        const u32 q1 = pk2(fma2(sp(gg[mt][1]), f32x2{u2f(s1[0]), u2f(s1[1])}, tv[mt][pp][h][1]));
                        const auto s2 = __builtin_amdgcn_permlane32_swap(q0, q1, false, false);
                        v[2 * h] = s2[0]; v[2 * h + 1] = s2[1];
                    }
                    st128(v, rs, tok * 1280 + n * 16 + pp * 256, WS_R + pair0 * 256);
                }
        }
        if (wid == 0 && l < 32) {
            const int tok = 64 * (f >> 1) + 2 * (16 * ((l >> 2) & 1) + 4 * (l >> 3) + (l & 3)) + (f & 1);
            st32(*(const LAS u32*)(lds + L_GS + l * 8 + 4), rs, tok * 4, WS_OG);
        }
    }
    STAMP(15);
    WAIT_VM(0);
    BAR();
    if (wid == 0 && l == 0) set_flag(ws, FL_F3 + f, tag);
}

__global__ __launch_bounds__(512, 2) void fused(Args A) {
    extern __shared__ __attribute__((aligned(16))) char lds[];
    const int l = threadIdx.x & 63, wid = __builtin_amdgcn_readfirstlane(threadIdx.x >> 6);
    const int xcd = blockIdx.x & 7, j = blockIdx.x >> 3;
    char* ws = A.ws;
    STAMP(0);
    const u32 epraw = __hip_atomic_load((GAS u32*)(ws + WS_EP), RLX_AGENT);
    if (j < 20) s_chain(A, lds, xcd, j, wid, l, epraw);
    else f_chain(A, lds, (j - 20) * 8 + xcd, wid, l, epraw);
}

extern "C" void kernel_launch(void* const* d_in, const int* in_sizes, int n_in, void* d_out, int out_size, void* d_ws, size_t ws_size,
                              hipStream_t stream) {
    Args A;
    A.x = (const u16*)d_in[0]; A.wr = (const u16*)d_in[1]; A.wi = (const u16*)d_in[2];
    A.lre = (const u16*)d_in[4]; A.lim = (const u16*)d_in[5]; A.bre = (const u16*)d_in[6]; A.bim = (const u16*)d_in[7];
    A.cre = (const u16*)d_in[8]; A.cim = (const u16*)d_in[9]; A.ldt = (const u16*)d_in[10]; A.dsk = (const u16*)d_in[11]; A.gw = (const u16*)d_in[12];
    A.ws = (char*)d_ws; A.out = (float*)d_out;
    (void)hipFuncSetAttribute((const void*)fused, hipFuncAttributeMaxDynamicSharedMemorySize, 163840);
    fused<<<256, 512, 163840, stream>>>(A);
}
```

```cpp
#include <hip/hip_runtime.h>

typedef unsigned short u16;
typedef unsigned int u32;
typedef unsigned long long u64;
typedef __bf16 bf16x8 __attribute__((ext_vector_type(8)));
typedef __bf16 bf16x2 __attribute__((ext_vector_type(2)));
typedef short s16x4 __attribute__((ext_vector_type(4)));
typedef float f32x2 __attribute__((ext_vector_type(2)));
typedef float f32x4 __attribute__((ext_vector_type(4)));
typedef u32 u32x2 __attribute__((ext_vector_type(2)));
typedef u32 u32x4 __attribute__((ext_vector_type(4)));
#define DI __device__ __forceinline__
#define LAS __attribute__((address_space(3)))
#define GAS __attribute__((address_space(1)))

namespace tab {
struct Frag { u16 v[512]; };
struct F50 { Frag f[50]; };
struct F32 { Frag f[32]; };
#if defined(__HIP_DEVICE_COMPILE__)
constexpr double PI = 3.14159265358979323846264338327950288;
constexpr double tsin(double x) { double t = x, s = x; for (int i = 1; i < 14; ++i) { t *= -x * x / ((2 * i) * (2 * i + 1)); s += t; } return s; }
constexpr double tcos(double x) { double t = 1, s = 1; for (int i = 1; i < 14; ++i) { t *= -x * x / ((2 * i - 1) * (2 * i)); s += t; } return s; }
constexpr double cosn(int i, int n) {
    i %= n; if (i < 0) i += n;
    if (2 * i > n) i = n - i;
    if (4 * i > n) return -cosn(n / 2 - i, n);
    if (8 * i > n) return tsin(2 * PI * (n / 4 - i) / n);
    return tcos(2 * PI * i / n);
}
constexpr u16 bf(double v) { float f = (float)v; u32 u = __builtin_bit_cast(u32, f); u += 0x7fffu + ((u >> 16) & 1u); return (u16)(u >> 16); }
struct CT { u16 c[160], c2[160], e[64]; };
constexpr CT mkct() { CT r{}; for (int i = 0; i < 160; ++i) { double c = cosn(i, 160); r.c[i] = bf(c); r.c2[i] = bf(2 * c); } for (int i = 0; i < 64; ++i) r.e[i] = bf(cosn(i, 64)); return r; }
constexpr CT ct = mkct();
constexpr F50 mkI1() {
    F50 t{};
    for (int ks = 0; ks < 5; ++ks)
        for (int ph = 0; ph < 10; ++ph)
            for (int l = 0; l < 64; ++l)
                for (int i = 0; i < 8; ++i) {
                    int k0 = 16 * ks + 8 * (i >> 2) + 2 * (l >> 4) + ((i >> 1) & 1), part = i & 1, j = 32 * (ph >> 1) + 2 * (l & 8) + 8 * (ph & 1) + (l & 7);
                    t.f[ks * 10 + ph].v[l * 8 + i] = k0 ? ct.c2[(j * k0 + 40 * part) % 160] : ct.c[j * 80 * part % 160];
                }
    return t;
}
constexpr u16 e64(int i) { return ct.e[((i % 64) + 64) % 64]; }
constexpr F32 mkT() {
    F32 f{};
    for (int l = 0; l < 64; ++l)
        for (int j = 0; j < 8; ++j) {
            int m = l & 15, g = l >> 4, perm = 16 * (j >> 2) + 4 * g + (j & 3), o = l * 8 + j;
            for (int a = 0; a < 2; ++a) {
                int x = (8 * g + j) * (16 * a + m) * 2, r = 16 * a + m;
                f.f[3 * a + 0].v[o] = e64(x); f.f[3 * a + 1].v[o] = e64(x - 16); f.f[3 * a + 2].v[o] = e64(x + 16);
                x = 2 * r * perm;
                f.f[6 + 3 * a].v[o] = e64(x); f.f[7 + 3 * a].v[o] = e64(x - 16); f.f[8 + 3 * a].v[o] = e64(x + 16);
                x = r * (2 * perm + 1);
                f.f[12 + 3 * a].v[o] = e64(x); f.f[13 + 3 * a].v[o] = e64(x - 16); f.f[14 + 3 * a].v[o] = e64(x + 16);
                x = perm * (2 * r + 1);
                f.f[18 + 3 * a].v[o] = e64(x); f.f[19 + 3 * a].v[o] = e64(x - 16); f.f[20 + 3 * a].v[o] = e64(x + 16);
                for (int w = 0; w < 2; ++w) {
                    int y = (16 * w + m) * (16 * a + 4 * g + (j & 3)) * 2;
                    f.f[24 + 4 * w + 2 * a].v[o] = e64(j < 4 ? y : y + 16);
                    f.f[25 + 4 * w + 2 * a].v[o] = e64(j < 4 ? y - 16 : y);
                }
            }
        }
    return f;
}
#endif
}
#if defined(__HIP_DEVICE_COMPILE__)
static __device__ __attribute__((aligned(1024))) const tab::F50 g_I = tab::mkI1();
static __device__ __attribute__((aligned(1024))) const tab::F32 g_T = tab::mkT();
#else
static __device__ const tab::F50 g_I = {};
static __device__ const tab::F32 g_T = {};
#endif

constexpr int WS_HF = 0;
constexpr int WS_XF = WS_HF + 160 * 32768;
constexpr int WS_WT = WS_XF + 320 * 8192;
constexpr int WS_GT = WS_WT + 324 * 8192;
constexpr int WS_R = WS_GT + 64 * 40960;
constexpr int WS_OG = WS_R + 2048 * 1280;
constexpr int WS_GR = WS_OG + 16384;
constexpr int FSTR = 128;
constexpr int WS_FL = WS_GR + 10 * 16 * 1024;
constexpr int FL_S1 = 0, FL_F1 = 160, FL_G = 256, FL_F3 = 320;
constexpr int WS_EP = WS_FL + 512 * 128;
constexpr int WS_END = WS_EP + 256;

typedef __amdgpu_buffer_rsrc_t rsrc_t;
DI void glds16(const void* g, int l) { asm volatile("s_mov_b32 m0, %0\n s_nop 0\n global_load_lds_dwordx4 %1, off" ::"s"(l), "v"(g) : "memory"); }
DI void glds16s(const void* base, int off, int l) { asm volatile("s_mov_b32 m0, %0\n s_nop 0\n global_load_lds_dwordx4 %1, %2" ::"s"(l), "v"(off), "s"(base) : "memory"); }
constexpr int L_DUM = 158 * 1024;
DI void touch(const void* base, int off) { asm volatile("s_mov_b32 m0, %0\n s_nop 0\n global_load_lds_dword %1, %2" ::"s"(L_DUM), "v"(off), "s"(base) : "memory"); }
#define GLD64(dst, p, o) asm volatile("global_load_dwordx2 %0, %1, off offset:%2" : "=v"(dst) : "v"(p), "i"(o))
#define GLD16(dst, p) asm volatile("global_load_ushort %0, %1, off" : "=v"(dst) : "v"(p))
#define USE(x) asm volatile("" : "+v"(x))
DI u32x4 ld128(rsrc_t r, int v, int s) { return __builtin_amdgcn_raw_buffer_load_b128(r, v, s, 16); }
DI u32x2 ld64(rsrc_t r, int v, int s) { return __builtin_amdgcn_raw_buffer_load_b64(r, v, s, 16); }
DI u32 ld32(rsrc_t r, int v, int s) { return __builtin_amdgcn_raw_buffer_load_b32(r, v, s, 16); }
DI void st128(u32x4 x, rsrc_t r, int v, int s) {
    __builtin_amdgcn_raw_buffer_store_b128(x, r, v, s, 16);
    asm volatile("s_nop 1" ::"v"(x) : "memory");
}
DI void st32(u32 x, rsrc_t r, int v, int s) { __builtin_amdgcn_raw_buffer_store_b32(x, r, v, s, 16); }
DI u32 pk2(float a, float b) { f32x2 v = {a, b}; return __builtin_bit_cast(u32, __builtin_convertvector(v, bf16x2)); }
DI u32 pk2(f32x2 v) { return __builtin_bit_cast(u32, __builtin_convertvector(v, bf16x2)); }
DI u32 f2u(float f) { return __builtin_bit_cast(u32, f); }
DI float u2f(u32 u) { return __builtin_bit_cast(float, u); }
DI float bflo(u32 u) { return __builtin_bit_cast(float, u << 16); }
DI float bfhi(u32 u) { return __builtin_bit_cast(float, u & 0xffff0000u); }
DI float bf2f(u16 u) { return __builtin_bit_cast(float, (u32)u << 16); }
DI float rbf(float a) { return bflo(pk2(a, 0.f)); }
DI bf16x8 ldsv(const char* p) { return *(const LAS bf16x8*)(p); }
DI bf16x8 asfrag(u32x4 v) { return __builtin_bit_cast(bf16x8, v); }
DI bf16x8 asfrag(u32 a, u32 b, u32 c, u32 d) { u32x4 v = {a, b, c, d}; return __builtin_bit_cast(bf16x8, v); }
DI bf16x8 trfrag(const char* p0, const char* p1) {
    s16x4 lo = __builtin_amdgcn_ds_read_tr16_b64_v4i16((LAS s16x4*)p0);
    s16x4 hi = __builtin_amdgcn_ds_read_tr16_b64_v4i16((LAS s16x4*)p1);
    return __builtin_bit_cast(bf16x8, __builtin_shufflevector(lo, hi, 0, 1, 2, 3, 4, 5, 6, 7));
}
DI void pair8(u32x4& a, u32x4& b) {
#pragma unroll
    for (int k = 0; k < 4; ++k) {
        const u32 x = a[k], y = b[k];
        a[k] = __builtin_amdgcn_update_dpp(x, y, 0x128, 0xf, 0xc, false);
        b[k] = __builtin_amdgcn_update_dpp(y, x, 0x128, 0xf, 0x3, false);
    }
}
DI f32x2 lo2(f32x4 v) { return __builtin_shufflevector(v, v, 0, 1); }
DI f32x2 hi2(f32x4 v) { return __builtin_shufflevector(v, v, 2, 3); }
DI f32x4 cat(f32x2 a, f32x2 b) { return __builtin_shufflevector(a, b, 0, 1, 2, 3); }
DI f32x2 sp(float a) { return f32x2{a, a}; }
DI f32x2 fma2(f32x2 a, f32x2 b, f32x2 c) { return __builtin_elementwise_fma(a, b, c); }
#define MFMA16(a, b, c) __builtin_amdgcn_mfma_f32_16x16x32_bf16((a), (b), (c), 0, 0, 0)
#define DSR(dst, addr, off) asm volatile("ds_read_b128 %0, %1 offset:%2" : "=v"(dst) : "v"(addr), "i"(off))
#define WAIT_VM(n) asm volatile("s_waitcnt vmcnt(%0)" ::"i"(n) : "memory")
#define WAIT_LGKM0() asm volatile("s_waitcnt lgkmcnt(0)" ::: "memory")
#define BAR() do { asm volatile("" ::: "memory"); __builtin_amdgcn_s_barrier(); asm volatile("" ::: "memory"); } while (0)
#define PIN2(x) asm volatile("" ::"v"(x))
#define SCHED() __builtin_amdgcn_sched_barrier(0)
#define RLX_AGENT __ATOMIC_RELAXED, __HIP_MEMORY_SCOPE_AGENT
DI f32x2 cmul(f32x2 a, f32x2 b) { return f32x2{a[0] * b[0] - a[1] * b[1], a[0] * b[1] + a[1] * b[0]}; }
DI f32x2 cfma(f32x2 a, f32x2 b, f32x2 c) { return f32x2{fmaf(a[0], b[0], fmaf(-a[1], b[1], c[0])), fmaf(a[0], b[1], fmaf(a[1], b[0], c[1]))}; }

constexpr int SPIN_MAX = 1 << 18;
constexpr u32 BLANK = 0xffffffffu;
DI u32 max3(u32 a, u32 b, u32 c) { return max(max(a, b), c); }
DI u32 maxw(u32 m, u32x4 v) { return max3(max3(m, v[0], v[1]), v[2], v[3]); }
DI u32 maxw(u32 m, u32x2 v) { return max3(m, v[0], v[1]); }
DI bool blank(u32 m) { return __builtin_expect(__any(m == BLANK), 0); }
DI u32 get_tag(const char* ws) {
    u32 v;
    asm volatile("s_load_dword %0, %1, %2\n s_waitcnt lgkmcnt(0)" : "=s"(v) : "s"(ws), "s"(WS_EP) : "memory");
    return v * 1664525u + 1013904223u;
}
DI u64 flagval(u32 tag) { return ((u64)tag << 32) | (tag * 22695477u + 1u); }
DI void set_flag(char* ws, int idx, u32 tag) { __hip_atomic_store((GAS u64*)(ws + WS_FL + idx * FSTR), flagval(tag), RLX_AGENT); }
DI bool wait_flags(char* ws, int i0, int i1, bool need1, u32 tag) {
    GAS u64 *p0 = (GAS u64*)(ws + WS_FL + i0 * FSTR), *p1 = (GAS u64*)(ws + WS_FL + i1 * FSTR);
    const u64 want = flagval(tag);
    bool ok1 = false;
    for (int spin = 0; spin < SPIN_MAX; ++spin) {
        const u64 v0 = __hip_atomic_load(p0, RLX_AGENT), v1 = __hip_atomic_load(p1, RLX_AGENT);
        ok1 = __all(v1 == want);
        if (__all(v0 == want) && (ok1 || !need1)) break;
        __builtin_amdgcn_s_sleep(1);
    }
    __builtin_amdgcn_fence(__ATOMIC_ACQUIRE, "wavefront");
    return ok1;
}
DI void wait_flags(char* ws, int i0, u32 tag) {
    GAS u64* p0 = (GAS u64*)(ws + WS_FL + i0 * FSTR);
    const u64 want = flagval(tag);
    for (int spin = 0; spin < SPIN_MAX; ++spin) {
        if (__all(__hip_atomic_load(p0, RLX_AGENT) == want)) break;
        __builtin_amdgcn_s_sleep(1);
    }
    __builtin_amdgcn_fence(__ATOMIC_ACQUIRE, "wavefront");
}

struct Args {
    const u16 *x, *wr, *wi, *lre, *lim, *bre, *bim, *cre, *cim, *ldt, *dsk, *gw;
    char* ws;
    float* out;
};

constexpr int STG = 32768;
constexpr int L_TB = 32768;
constexpr int L_AGG = 152 * 1024, L_HP = L_AGG + 4096;
constexpr int L_PAR = 128 * 1024;
constexpr int NE9 = -4;
constexpr int PFD = 5;
constexpr int L_GW = L_PAR + 12 * 1024;
constexpr int RFL = 17, RLD = 19;
constexpr int CX3 = 8;
constexpr int cslot(int i) { return i < 8 ? 8 + i : i < 12 ? i - 8 : i < 15 ? i + 4 : i < 19 ? i - 11 : 19; }

DI void s_chain(const Args& A, char* lds, int xcd, int j, int wid, int l) {
    char* ws = A.ws;
    const int m = 4 * (xcd >> 1) + (j & 3), ct = 5 * (xcd & 1) + (j >> 2), dt = ct;
    const rsrc_t rs = __builtin_amdgcn_make_buffer_rsrc(ws, 0, WS_END, 0x00020000);
    const int n = l & 15, g = l >> 4;
    u32 tag;
    const int tp = wid & 3, kh = wid >> 2;
    const int tile = 2 * tp + kh, token = 128 * m + 16 * tile + 8 * (n & 1) + (n >> 1);
    f32x4 acc2[4][2];
#pragma unroll
    for (int i = 0; i < 8; ++i) acc2[i >> 1][i & 1] = f32x4{0.f, 0.f, 0.f, 0.f};
    int ctl[10];
    {
        const int a = j >> 2, hm = dt - a, fo = 5 - hm;
        auto m5 = [](int v) { return v >= 5 ? v - 5 : v; };
        ctl[0] = dt; ctl[1] = fo + a;
#pragma unroll
        for (int s = 2; s < 7; ++s) ctl[s] = s == a + 2 ? hm + m5(a + 1) : fo + m5(a + (s < a + 2 ? s - 1 : s - 2));
#pragma unroll
        for (int s = 7; s < 10; ++s) ctl[s] = hm + m5(a + s - 5);
    }
    const int bo = (8 * tp + (l >> 3)) * 1280 + ((l & 7) ^ ((4 * tp + g) & 7)) * 16;
    auto issue_c = [&](int i) {
        const int c2 = ctl[i >> 1];
        const char* src = (const char*)((i & 1) ? A.cim : A.cre) + 64 * dt * 1280 + c2 * 128;
        const int d = cslot(i) * 8192 + tp * 1024;
        glds16s(src, bo, d); glds16s(src + 32 * 1280, bo, d + 4096);
    };
    if (wid >= 4) {
        const int w = wid - 4, rl = 8 * w + (l >> 3);
        const int k0 = (2 * (j >> 2) + 3 * (j & 3)) % 10;
        const int a0 = rl * 1280 + ((l & 7) ^ w) * 16, a1 = a0 ^ 64;
        auto issue = [&](int st) {
            int kk = st + k0; if (kk >= 10) kk -= 10;
            const int d = (st & 3) * STG + w * 1024;
            const char *pa = (const char*)A.x + 128 * m * 1280 + kk * 128, *pb0 = (const char*)A.bre + 64 * ct * 1280 + kk * 128, *pb1 = (const char*)A.bim + 64 * ct * 1280 + kk * 128;
            glds16s(pa, a0, d); glds16s(pb0, bo, d + 16384);
            glds16s(pa + 32 * 1280, a1, d + 4096); glds16s(pb0 + 32 * 1280, bo, d + 20480);
            glds16s(pa + 64 * 1280, a0, d + 8192); glds16s(pb1, bo, d + 24576);
            glds16s(pa + 96 * 1280, a1, d + 12288); glds16s(pb1 + 32 * 1280, bo, d + 28672);
        };
        issue(0);
        issue(1);
        u32x2 xv[2][4], dv[4], gx[10], gw[10];
        tag = get_tag(ws);
        const int grow = 16 * (ct & 7) + 4 * w + g;
        const char* gp = lds + grow * 128 + (((n >> 1) ^ (grow >> 3)) & 7) * 16 + (n & 1) * 8;
        const int xrow = 16 * tile + 8 * (n & 1) + (n >> 1), sts = dt >= k0 ? dt - k0 : dt - k0 + 10;
        u32 xa[4][2];
#pragma unroll
        for (int i = 0; i < 8; ++i) {
            const int d = i >> 1, r = xrow - 16 * (i & 1);
            xa[d][i & 1] = (sts & 3) * STG + r * 128 + (((2 * d + (g >> 1)) ^ (r >> 3)) & 7) * 16 + (g & 1) * 8;
            USE(xa[d][i & 1]);
        }
#pragma unroll
        for (int st = 0; st < 10; ++st) {
            WAIT_VM(8);
            WAIT_LGKM0();
            BAR();
            gx[st] = *(const LAS u32x2*)(gp + (st & 3) * STG);
            if (st == sts) {
#pragma unroll
                for (int i = 0; i < 8; ++i) xv[i & 1][i >> 1] = *(const LAS u32x2*)(size_t)(xa[i >> 1][i & 1]);
            }
            if (st + 2 < 10) issue(st + 2);
            if (st == 8) { issue_c(0); issue_c(1); issue_c(2); issue_c(3); }
            if (st == 9) {
#pragma unroll
                for (int i = 4; i < 8 + NE9; ++i) issue_c(i);
            }
        }
#pragma unroll
        for (int st = 0; st < 10; ++st) {
            int kk = st + k0; if (kk >= 10) kk -= 10;
            gw[st] = *(const LAS u32x2*)(lds + L_GW + kk * 128 + n * 8); USE(gw[st]);
        }
#pragma unroll
        for (int d = 0; d < 4; ++d) { dv[d] = *(const LAS u32x2*)(lds + L_GW + 1280 + d * 32 + g * 8); USE(dv[d]); }
        const char* pp = lds + L_PAR + l * 16 + 7168;
        const f32x2 l16 = *(const LAS f32x2*)(pp + 8), l32 = *(const LAS f32x2*)(pp + 1024), l64 = *(const LAS f32x2*)(pp + 2048);
        const f32x2 l128 = cmul(l64, l64), l256 = cmul(l128, l128), l512 = cmul(l256, l256);
        f32x2 lw = w & 1 ? l128 : f32x2{1.f, 0.f};
        if (w & 2) lw = cmul(lw, l256);
        PIN2(lw); PIN2(l512); PIN2(l16); PIN2(l32);
        BAR();
        const int gro = WS_GR + ct * 16384;
        if (w == 3 && m < 15) {
            f32x2 e[8];
#pragma unroll
            for (int sg = 0; sg < 8; ++sg) e[sg] = *(const LAS f32x2*)(lds + L_AGG + sg * 512 + l * 8);
            const f32x2 t = cfma(l64, cfma(l32, cfma(l16, e[0], e[1]), cfma(l16, e[2], e[3])), cfma(l32, cfma(l16, e[4], e[5]), cfma(l16, e[6], e[7])));
            st128(u32x4{f2u(t[0]), tag, f2u(t[1]), tag}, rs, l * 16, gro + m * 1024);
        }
        {
            u32x4 ev[4];
            for (int spin = 0; spin < SPIN_MAX; ++spin) {
                bool ok = true;
                asm volatile("" ::: "memory");
#pragma unroll
                for (int i = 0; i < 4; ++i) {
                    const int mp = m - 1 - w - 4 * i;
                    ev[i] = u32x4{0u, tag, 0u, tag};
                    if (mp >= 0) ev[i] = ld128(rs, l * 16, gro + mp * 1024);
                }
#pragma unroll
                for (int i = 0; i < 4; ++i) ok &= ev[i][1] == tag && ev[i][3] == tag;
                if (__all(ok)) break;
                __builtin_amdgcn_s_sleep(1);
            }
            f32x2 h = {u2f(ev[3][0]), u2f(ev[3][2])};
#pragma unroll
            for (int i = 2; i >= 0; --i) h = cfma(l512, h, f32x2{u2f(ev[i][0]), u2f(ev[i][2])});
            *(LAS f32x2*)(lds + L_HP + w * 512 + l * 8) = cmul(h, lw);
            WAIT_LGKM0();
            WAIT_VM(0);
        }
        BAR();
        float gsum = 0.f;
#pragma unroll
        for (int i = 0; i < 20; ++i) {
            const u32 a = gx[i >> 1][i & 1], b = gw[i >> 1][i & 1], pp = pk2(bflo(a) * bflo(b), bfhi(a) * bfhi(b));
            gsum = __builtin_amdgcn_fdot2_f32_bf16(__builtin_bit_cast(bf16x2, pp), __builtin_bit_cast(bf16x2, 0x3f803f80u), gsum, false);
        }
        gsum += __shfl_xor(gsum, 1); gsum += __shfl_xor(gsum, 2); gsum += __shfl_xor(gsum, 4); gsum += __shfl_xor(gsum, 8);
        const float gt = rbf(1.f / (1.f + expf(-rbf(gsum))));
        if (n < 2 && ct < 8) st32(f2u(n ? gt * (1.f / (2048.f * 640.f)) : rbf(1.f - gt)), rs, (128 * m + grow) * 4 + n * 8192, WS_OG);
#pragma unroll
        for (int i = 0; i < 8; ++i) {
            const int d = i >> 1, t = i & 1;
            const u32 p0 = pk2(bflo(xv[t][d][0]) * bflo(dv[d][0]), bfhi(xv[t][d][0]) * bfhi(dv[d][0]));
            const u32 p1 = pk2(bflo(xv[t][d][1]) * bflo(dv[d][1]), bfhi(xv[t][d][1]) * bfhi(dv[d][1]));
            acc2[d][t] = f32x4{bflo(p0), bfhi(p0), bflo(p1), bfhi(p1)};
            USE(acc2[d][t]);
        }
#pragma unroll
        for (int i = 8 + NE9; i < CX3; ++i) issue_c(i);
        BAR();
    } else {
        const int wm = wid >> 1, wn = wid & 1, seg = 4 * wm + g;
        if (wid < 2) {
            const float ar = bf2f(A.lre[64 * ct + l]), ai = bf2f(A.lim[64 * ct + l]), dl = rbf(expf(bf2f(A.ldt[64 * ct + l])));
            const float ex = expf(dl * ar), rv = __builtin_amdgcn_fractf(dl * ai * 0.15915494309189535f);
            const f32x2 lam = {ex * __builtin_amdgcn_cosf(rv), ex * __builtin_amdgcn_sinf(rv)};
            const f32x2 l2 = cmul(lam, lam), l4 = cmul(l2, l2), l8 = cmul(l4, l4), l16 = cmul(l8, l8);
            char* po = lds + L_PAR + l * 16;
            if (wid == 0) {
                const f32x2 l32 = cmul(l16, l16), l64 = cmul(l32, l32), l48 = cmul(l32, l16);
                *(LAS f32x4*)(po) = cat(lam, l8);
                *(LAS f32x4*)(po + 7168) = cat(f32x2{1.f, 0.f}, l16); *(LAS f32x4*)(po + 8192) = cat(l32, l48);
                *(LAS f32x4*)(po + 9216) = cat(l64, cmul(l64, l16)); *(LAS f32x4*)(po + 10240) = cat(cmul(l64, l32), cmul(l64, l48));
            } else {
                const float den = 2.f / (ar * ar + ai * ai), nr = lam[0] - 1.f;
                const f32x2 kap = {(nr * ar + lam[1] * ai) * den, (lam[1] * ar - nr * ai) * den};
                *(LAS f32x4*)(po + 1024) = cat(l16, kap);
                f32x2 qa = cmul(kap, lam), qb = cmul(kap, l2);
#pragma unroll
                for (int i = 0; i < 4; ++i) { *(LAS f32x4*)(po + 3072 + i * 1024) = cat(qa, qb); qa = cmul(qa, l2); qb = cmul(qb, l2); }
            }
        }
        if (wid >= 2) {
            if (wid == 2) glds16((const char*)A.gw + l * 16, L_GW);
            else if (l < 24) glds16(l < 16 ? (const char*)A.gw + 1024 + l * 16 : (const char*)A.dsk + 128 * dt + (l - 16) * 16, L_GW + 1024);
        }
        tag = get_tag(ws);
        const int so = WS_HF + (m * 10 + ct) * 32768 + wn * 16384 + wm * 4096;
        f32x4 acc[4][4], pl[2];
        const char* pp = lds + L_PAR + (32 * wn + n) * 16;
#pragma unroll
        for (int i = 0; i < 16; ++i) acc[i >> 2][i & 3] = f32x4{0.f, 0.f, 0.f, 0.f};
        {
            const int rowA = 64 * wm + 16 * (n >> 2) + 8 * (n & 1) + ((n >> 1) & 1), swA = 2 * (n >> 2) + (n & 1);
            int aa[2][2], ba[2][2];
#pragma unroll
            for (int ks = 0; ks < 2; ++ks) {
                aa[0][ks] = rowA * 128 + (((4 * ks + g) ^ swA) * 16); ba[0][ks] = 16384 + (32 * wn + n) * 128 + (((4 * ks + g) ^ (n >> 1)) * 16);
                aa[1][ks] = aa[0][ks] + 65536; ba[1][ks] = ba[0][ks] + 65536;
            }
            bf16x8 fa[2][4], fb[2][4];
#pragma unroll
            for (int st = 0; st < 10; ++st) {
                WAIT_LGKM0();
                if (st == 4) WAIT_VM(0);

                BAR();
                if (st == 0)
                if (st == 5)
                SCHED();
#pragma unroll
                for (int ks = 0; ks < 2; ++ks) {
#pragma unroll
                    for (int i = 0; i < 16; ++i) {
                        if (st + ks > 0) acc[i >> 2][i & 3] = MFMA16(fa[ks ^ 1][i >> 2], fb[ks ^ 1][i & 3], acc[i >> 2][i & 3]);
                        SCHED();
                        const int rd = i < 4 ? i : (i < 12 && !(i & 1)) ? 2 + i / 2 : -1;
                        if (rd >= 0 && rd < 4) DSR(fb[ks][rd], ba[(st >> 1) & 1][ks], (st & 1) * STG + (rd >> 1) * 2048 + (rd & 1) * 8192);
                        if (rd >= 4) DSR(fa[ks][rd - 4], aa[(st >> 1) & 1][ks], (st & 1) * STG + (rd - 4) * 256);
                        SCHED();
                    }
                    if (ks == 0) { WAIT_LGKM0(); SCHED(); }
                }
            }
            WAIT_LGKM0();
            SCHED();
            pl[0] = *(const LAS f32x4*)(pp); pl[1] = *(const LAS f32x4*)(pp + 256);
            SCHED();
#pragma unroll
            for (int i = 0; i < 16; ++i) acc[i >> 2][i & 3] = MFMA16(fa[1][i >> 2], fb[1][i & 3], acc[i >> 2][i & 3]);
        }
        const f32x2 lam[2] = {lo2(pl[0]), lo2(pl[1])}, l8[2] = {hi2(pl[0]), hi2(pl[1])};
        f32x2 ur[2][8], ui[2][8], ea[2];
#pragma unroll
        for (int a = 0; a < 2; ++a) {
#pragma unroll
            for (int t = 0; t < 8; ++t) {
                ur[a][t] = (t & 1) ? hi2(acc[t >> 1][2 * a]) : lo2(acc[t >> 1][2 * a]);
                ui[a][t] = (t & 1) ? hi2(acc[t >> 1][2 * a + 1]) : lo2(acc[t >> 1][2 * a + 1]);
            }
        }
#pragma unroll
        for (int t = 1; t < 8; ++t)
#pragma unroll
            for (int a = 0; a < 2; ++a) {
                ur[a][t] = fma2(sp(lam[a][0]), ur[a][t - 1], fma2(sp(-lam[a][1]), ui[a][t - 1], ur[a][t]));
                ui[a][t] = fma2(sp(lam[a][0]), ui[a][t - 1], fma2(sp(lam[a][1]), ur[a][t - 1], ui[a][t]));
            }
        char* agp = lds + L_AGG + (32 * wn + n) * 8;
#pragma unroll
        for (int a = 0; a < 2; ++a) {
            ea[a] = f32x2{ur[a][7][0], ui[a][7][0]};
            *(LAS f32x2*)(agp + seg * 512 + a * 128) = cfma(l8[a], ea[a], f32x2{ur[a][7][1], ui[a][7][1]});
        }
        WAIT_LGKM0();
        BAR();
        f32x2 pre[2], lsg[2], q[2][8];
#pragma unroll
        for (int a = 0; a < 2; ++a) {
            const f32x4 pk = *(const LAS f32x4*)(pp + a * 256 + 1024);
            const f32x2 l16 = lo2(pk), kap = hi2(pk);
#pragma unroll
            for (int i = 0; i < 4; ++i) { const f32x4 v = *(const LAS f32x4*)(pp + a * 256 + 3072 + i * 1024); q[a][2 * i] = lo2(v); q[a][2 * i + 1] = hi2(v); }
            lsg[a] = *(const LAS f32x2*)(pp + a * 256 + 7168 + (seg >> 1) * 1024 + (seg & 1) * 8);
            pre[a] = f32x2{0.f, 0.f};
#pragma unroll
            for (int sg = 0; sg < 7; ++sg) {
                const f32x2 nx = cfma(l16, pre[a], *(const LAS f32x2*)(agp + sg * 512 + a * 128));
                if (sg < seg) pre[a] = nx;
            }
#pragma unroll
            for (int t = 0; t < 8; ++t) {
                const f32x2 r = ur[a][t], i = ui[a][t];
                ur[a][t] = fma2(sp(kap[0]), r, sp(-kap[1]) * i);
                ui[a][t] = fma2(sp(-kap[0]), i, sp(-kap[1]) * r);
                PIN2(ur[a][t]); PIN2(ui[a][t]); PIN2(q[a][t]);
            }
            PIN2(pre[a]); PIN2(lsg[a]);
        }
        BAR();
        char* tb = lds + L_TB + wid * 8192;
        {
            const int sw = (n & 3) | ((((n >> 3) ^ (n >> 2)) & 1) << 2);
            f32x2 cr[2], ci[2];
#pragma unroll
            for (int a = 0; a < 2; ++a) {
                const char* hp = lds + L_HP + (32 * wn + 16 * a + n) * 8;
                const f32x2 hin = (*(const LAS f32x2*)(hp) + *(const LAS f32x2*)(hp + 512)) + (*(const LAS f32x2*)(hp + 1024) + *(const LAS f32x2*)(hp + 1536));
                const f32x2 ca = cfma(lsg[a], hin, pre[a]), cb = cfma(l8[a], ca, ea[a]);
                cr[a] = f32x2{ca[0], cb[0]}; ci[a] = f32x2{ca[1], cb[1]};
            }
            const int r = n >> 2;
            const int s0 = r | ((g & 1) << 2), c0 = (n >> 1) & 1;
            const char* rp = tb + (8 * g + r) * 128 + (n & 1) * 8;
            bf16x8 fr[4];
#pragma unroll
            for (int part = 0; part < 2; ++part) {
#pragma unroll
                for (int a = 0; a < 2; ++a) {
                    u32 pv[8];
#pragma unroll
                    for (int t = 0; t < 8; ++t)
                        pv[t] = part ? pk2(fma2(sp(-q[a][t][0]), ci[a], fma2(sp(-q[a][t][1]), cr[a], ui[a][t])))
                                     : pk2(fma2(sp(q[a][t][0]), cr[a], fma2(sp(-q[a][t][1]), ci[a], ur[a][t])));
#pragma unroll
                    for (int h = 0; h < 2; ++h)
                        *(LAS u32x4*)(tb + part * 4096 + (16 * a + n) * 128 + (((2 * g + h) ^ sw) * 16)) = u32x4{pv[4 * h], pv[4 * h + 1], pv[4 * h + 2], pv[4 * h + 3]};
                    if (part == 1 && a == 0) {
                        SCHED();
#pragma unroll
                        for (int tt = 0; tt < 4; ++tt) st128(__builtin_bit_cast(u32x4, fr[tt]), rs, l * 16 + tt * 1024, so);
                        SCHED();
                    }
                }
#pragma unroll
                for (int tt = 0; tt < 4; ++tt) fr[tt] = trfrag(rp + part * 4096 + (((2 * tt + c0) ^ s0) * 16), rp + part * 4096 + 512 + (((2 * tt + c0) ^ s0 ^ 4) * 16));
                SCHED();
            }
#pragma unroll
            for (int tt = 0; tt < 4; ++tt) st128(__builtin_bit_cast(u32x4, fr[tt]), rs, l * 16 + tt * 1024, so + 8192);
        }
        BAR();
    }
    const int hv0 = WS_HF + m * 327680 + kh * 16384 + tile * 1024 + l * 16, hv1 = hv0 + (kh ? -1024 : 1024);
    bf16x8 hf[PFD + 1][2], cf[2][4];
    auto ldh = [&](int i) {
        const int c2 = ctl[i >> 1];
        const int so = c2 * 32768 + (i & 1) * 8192;
        hf[i % (PFD + 1)][0] = asfrag(ld128(rs, hv0, so)); hf[i % (PFD + 1)][1] = asfrag(ld128(rs, hv1, so));
    };
    int ca[3];
    ca[0] = n * 128 + (((4 * kh + g) ^ (n >> 1)) * 16); ca[1] = ca[0] + 65536; ca[2] = ca[0] + 131072;
    auto step = [&](int i) {
        WAIT_LGKM0();
        SCHED();
#pragma unroll
        for (int k = 0; k < 8; ++k) {
            const int d = k >> 1, t = k & 1;
            acc2[d][t] = MFMA16(cf[i & 1][d], hf[i % (PFD + 1)][t], acc2[d][t]);
            SCHED();
            if (k < 4 && i + 1 < 20) DSR(cf[(i + 1) & 1][k], ca[cslot(i + 1) >> 3], (cslot(i + 1) & 7) * 8192 + k * 2048);
            SCHED();
        }
    };
    {
        const int r = n >> 2, s0 = r | ((g & 1) << 2), c0 = (n >> 1) & 1;
        const char* rp = lds + L_TB + (2 * (tp >> 1) + kh) * 8192 + (8 * g + r) * 128 + (n & 1) * 8;
#pragma unroll
        for (int i = 0; i < 4; ++i) {
            const int x = (2 * ((tile ^ (i & 1)) & 3) + c0) ^ s0;
            hf[i >> 1][i & 1] = trfrag(rp + (i >> 1) * 4096 + x * 16, rp + (i >> 1) * 4096 + 512 + (x ^ 4) * 16);
        }
#pragma unroll
        for (int d = 0; d < 4; ++d) DSR(cf[0][d], ca[cslot(0) >> 3], (cslot(0) & 7) * 8192 + d * 2048);
        WAIT_LGKM0();
        if (kh) WAIT_VM(2 * (CX3 - 8 - NE9)); else WAIT_VM(0);
        BAR();
    }
    if (wid == 0 && l == 0) set_flag(ws, FL_S1 + m * 10 + ct, tag);
    auto issue_h = [&](int i) {
        const char* src = (const char*)((i & 1) ? A.cim : A.cre) + (64 * dt + 32 * kh) * 1280 + ctl[i >> 1] * 128;
        glds16s(src, bo, cslot(i) * 8192 + wid * 1024);
    };
    if (wid != 1) {
#pragma unroll
        for (int i = CX3; i < 20; ++i) issue_h(i);
    }
    step(0); step(1);
    if (wid == 1) {
        wait_flags(ws, FL_S1 + m * 10 + (l < 10 ? l : 0), tag);
#pragma unroll
        for (int i = CX3; i < 20; ++i) issue_h(i);
    }
    if (kh) WAIT_VM(2 * (CX3 - 8) + (20 - CX3));
    BAR();
#pragma unroll
    for (int i = 2; i < 2 + PFD; ++i) ldh(i);
    const u64 want = flagval(tag);
    GAS u64* f3 = (GAS u64*)(ws + WS_FL + (FL_F3 + 4 * m + 2 * (tp >> 1) + (l & 1)) * FSTR);
    u64 fv = 0;
    bool have = false;
    u32x4 rv[2]; u32 og[2] = {0u, 0u};
    const int tk = 128 * m + 16 * tile + (l >> 3);
    const int ro = WS_R + tk * 1280 + dt * 128 + (l & 7) * 16;
    {
#pragma unroll
        for (int i = 2; i < 20; ++i) {
            if (i == 6) BAR();
            if (i > 2 && i + PFD - 1 < 20) ldh(i + PFD - 1);
            if (i == RFL) { fv = __hip_atomic_load(f3, RLX_AGENT); og[0] = ld32(rs, WS_OG + tk * 4, 0); og[1] = ld32(rs, WS_OG + tk * 4 + 32, 0); }
            if (i == RLD) {
                have = __all(fv == want);
                __builtin_amdgcn_fence(__ATOMIC_ACQUIRE, "wavefront");
#pragma unroll
                for (int d = 0; d < 2; ++d) rv[d] = ld128(rs, have ? ro : WS_END, d * 10240);
            }
            step(i);
        }
    }
    if (!have) fv = __hip_atomic_load(f3, RLX_AGENT);
#pragma unroll
    for (int d = 0; d < 4; ++d) *(LAS f32x4*)(lds + 65536 + wid * 4096 + d * 1024 + l * 16) = acc2[d][1];
    WAIT_LGKM0();
    BAR();
#pragma unroll
    for (int d = 0; d < 4; ++d) { acc2[d][0] += *(const LAS f32x4*)(lds + 65536 + (wid ^ 4) * 4096 + d * 1024 + l * 16); USE(acc2[d][0]); }
    if (!have) {
        have = __all(fv == want);
        __builtin_amdgcn_fence(__ATOMIC_ACQUIRE, "wavefront");
#pragma unroll
        for (int d = 0; d < 2; ++d) rv[d] = ld128(rs, have ? ro : WS_END, d * 10240);
    }
    char* xp = lds + 65536 + (wid ^ 4) * 4096;
#pragma unroll
    for (int d = 0; d < 4; ++d) *(LAS f32x4*)(xp + n * 256 + (((4 * d + g) ^ n) * 16)) = acc2[d][0];
    f32x4 y[4];
#pragma unroll
    for (int d = 0; d < 4; ++d) {
        const int r = 2 * (l >> 3) + (d >> 1);
        y[d] = *(const LAS f32x4*)(xp + r * 256 + (((8 * (d & 1) + (l & 7)) ^ r) * 16));
    }
#pragma unroll
    for (int d = 0; d < 4; ++d) USE(y[d]);
    const rsrc_t ro_ = __builtin_amdgcn_make_buffer_rsrc(A.out, 0, 2048 * 2560, 0x00020000);
    int op = tk * 2560 + 256 * dt + 16 * (l & 7);
    USE(op);
    if (!have) {
        for (int spin = 0; spin < SPIN_MAX; ++spin) {
            if (__all(__hip_atomic_load(f3, RLX_AGENT) == want)) break;
            __builtin_amdgcn_s_sleep(1);
        }
        __builtin_amdgcn_fence(__ATOMIC_ACQUIRE, "wavefront");
#pragma unroll
        for (int d = 0; d < 2; ++d) rv[d] = ld128(rs, ro, d * 10240);
    }
#pragma unroll
    for (int d = 0; d < 4; ++d) {
        const float w = u2f(og[d >> 1]);
        const u32 r0 = rv[d >> 1][2 * (d & 1)], r1 = rv[d >> 1][2 * (d & 1) + 1];
        const u32x4 o = {f2u(fmaf(w, y[d][0], bflo(r0))), f2u(fmaf(w, y[d][1], bfhi(r0))), f2u(fmaf(w, y[d][2], bflo(r1))), f2u(fmaf(w, y[d][3], bfhi(r1)))};
        __builtin_amdgcn_raw_buffer_store_b128(o, ro_, op + (d & 1) * 128, (d >> 1) * 8 * 2560, 18);
        asm volatile("s_nop 1" ::"v"(o) : "memory");
    }
    if (m == 15 && dt == 0 && wid == 0 && l == 0) __hip_atomic_store((GAS u32*)(ws + WS_EP), tag, RLX_AGENT);
}

constexpr int L_T = 51200, L_X1 = L_T + 32768;

constexpr int GSH = 35;
DI void f_chain(const Args& A, char* lds, int f, int wid, int l) {
    char* ws = A.ws;
    const rsrc_t rs = __builtin_amdgcn_make_buffer_rsrc(ws, 0, WS_END, 0x00020000);
    const int n = l & 15, g = l >> 4;
    const f32x4 z4 = {0.f, 0.f, 0.f, 0.f};
    const int t1 = f >> 4, gq = (f >> 1) & 3, quad = ((f >> 2) & 2) | (f & 1);
    bf16x8 fb[2][5];
    u32 tag;
    if (f < 64) {
        const char* xb = (const char*)A.x + (size_t)(64 * (8 * gq + wid) + 32 * (t1 & 1) + 8 * quad + (t1 >> 1)) * 1280;
#pragma unroll
        for (int it = 0; it < 5; ++it) {
            const int L = it * 64 + l, rr = (L * 205) >> 14, ch = L - rr * 80;
            glds16s(xb, rr * 2560 + ((ch ^ (rr << 2)) * 16), (wid * 5 + it) * 1024);
        }
        if (wid < 5) {
            const float k0 = (float)(16 * wid + n), st = k0 * 0.2f;
            const f32x2 rc = sp(__builtin_amdgcn_cosf(st)), rn = sp(__builtin_amdgcn_sinf(st));
            f32x2 co[4], si[4];
#pragma unroll
            for (int i = 0; i < 8; ++i) {
                const float th = k0 * (1.f / 160.f) * (float)(16 * (i >> 2) + 4 * g + (i & 3));
                co[i >> 1][i & 1] = __builtin_amdgcn_cosf(th); si[i >> 1][i & 1] = __builtin_amdgcn_sinf(-th);
            }
            const u32 alt = pk2(1.f, -1.f);
            const bool z = 16 * wid + n == 0;
#pragma unroll
            for (int ks = 0; ks < 5; ++ks) {
                fb[0][ks] = asfrag(pk2(co[0]), pk2(co[1]), pk2(co[2]), pk2(co[3]));
                fb[1][ks] = asfrag(z ? alt : pk2(si[0]), z ? alt : pk2(si[1]), z ? alt : pk2(si[2]), z ? alt : pk2(si[3]));
                PIN2(fb[0][ks]); PIN2(fb[1][ks]);
                if (ks < 4) {
#pragma unroll
                    for (int i = 0; i < 4; ++i) { const f32x2 c = co[i], s = si[i]; co[i] = fma2(c, rc, s * rn); si[i] = fma2(s, rc, -c * rn); }
                }
            }
        }
        if (wid >= 6) {
            const u16* pb = wid == 6 ? A.bre : A.bim;
            touch(pb, (f * 10 + (l >> 3)) * 1280 + 256 + (l & 7) * 128); touch(pb, (f * 10 + 8 + ((l >> 3) & 1)) * 1280 + 256 + (l & 7) * 128);
        }
        tag = get_tag(ws);
        if (wid >= 6) WAIT_VM(2); else WAIT_VM(0);
    } else {
        for (int i = wid; i < 82; i += 8) {
            const int arr = i >= 41, k = i - 41 * arr;
            int o = (f - 64) * 41088 + k * 1024 + l * 16;
            if (o >= 2048 * 642) o -= 2048 * 642;
            glds16s(arr ? A.wi : A.wr, o, (arr ? L_X1 : 0) + k * 1024);
        }
        tag = get_tag(ws);
        WAIT_VM(0);
    }
    BAR();
    if (f < 64) {
        const int so = WS_XF + t1 * 2048 + gq * 512 + quad * 128, vo = (n & 7) * 32768 + (n >> 3) * 64 + g * 16;
        if (wid < 5) {
            f32x4 acc[2][8];
            const int b = n & 3;
            const char* a0 = lds + b * 1280 + (((g >> 1) ^ b) * 64) + (4 * (g & 1) + (n >> 2)) * 8;
            const char* a1 = lds + b * 1280 + (((2 + (g >> 1)) ^ b) * 64) + (4 * (g & 1) + (n >> 2)) * 8;
            bf16x8 fa[2][8];
#pragma unroll
            for (int jx = 0; jx < 8; ++jx) fa[0][jx] = trfrag(a0 + jx * 5120, a1 + jx * 5120);
#pragma unroll
            for (int ks = 0; ks < 5; ++ks) {
#pragma unroll
                for (int jx = 0; jx < 8; ++jx) {
                    if (ks < 4) fa[(ks + 1) & 1][jx] = trfrag(a0 + jx * 5120 + (ks + 1) * 256, a1 + jx * 5120 + (ks + 1) * 256);
                    acc[0][jx] = MFMA16(fa[ks & 1][jx], fb[0][ks], ks ? acc[0][jx] : z4);
                    acc[1][jx] = MFMA16(fa[ks & 1][jx], fb[1][ks], ks ? acc[1][jx] : z4);
                    SCHED();
                }
            }
#pragma unroll
            for (int cl = 0; cl < 4; ++cl) {
                u32x4 v[2];
#pragma unroll
                for (int p = 0; p < 2; ++p) v[p] = u32x4{pk2(acc[p][0][cl], acc[p][1][cl]), pk2(acc[p][2][cl], acc[p][3][cl]), pk2(acc[p][4][cl], acc[p][5][cl]), pk2(acc[p][6][cl], acc[p][7][cl])};
                pair8(v[0], v[1]);
                st128(v[0], rs, vo, so + wid * 16 * 32768 + cl * 8192);
                st128(v[1], rs, vo, so + (wid * 16 + 8) * 32768 + cl * 8192);
            }
        } else {
            for (int i = wid - 5; i < 32; i += 3) glds16((const char*)&g_T + i * 1024 + l * 16, L_T + i * 1024);
            if (wid >= 6) {
                __builtin_amdgcn_s_sleep(24);
                const u16* pc = wid == 6 ? A.cre : A.cim;
                touch(pc, (f * 100 + l) * 128); touch(pc, (f * 100 + 64 + min(l, 35)) * 128);
            } else if (wid == 5) touch(&g_I, min((f >> 3) * 64 + l, 399) * 128);
        }
    } else {
        if (f <= 80) { for (int i = wid; i < 32; i += 8) glds16((const char*)&g_T + i * 1024 + l * 16, L_T + i * 1024); }
        const int sl = 16 * (n >> 3) + 2 * (n & 7);
        const int o0 = (((f >> 3) & 1) * 4 + (f & 1) * 2 + (n >> 3)) * 1024 + ((f >> 4) & 1) * 512 + ((f >> 1) & 3) * 128 + (n & 7) * 16;
        const int o1 = o0 ^ (6 * 1024 + 512 + 3 * 128);
#pragma unroll
        for (int it = 0; it < 11; ++it) {
            const int fi = wid * 4 + g + 32 * it;
            const bool mir = fi >= 162;
            const int col = mir ? (fi >= 243 ? 403 : 482) - fi : (fi >= 81 ? 79 : 0) + fi;
            const char* p = lds + col * 2 + (mir ? 64 - sl : sl) * 642;
            const int st = mir ? -642 : 642;
            if (it < 10 || wid == 0) {
                u32x4 v;
#pragma unroll
                for (int k = 0; k < 4; ++k) {
                    const char* pk = p + ((k & 1) * 32 + (k >> 1)) * st;
                    v[k] = (u32)*(const LAS u16*)(pk) | ((u32)*(const LAS u16*)(pk + L_X1) << 16);
                }
                st128(v, rs, fi * 8192 + (mir ? o1 : o0), WS_WT);
            }
        }
    }
    const int c = wid >> 1, e = wid & 1;
    const float ph = (float)(c * f) * (1.f / 640.f);
    f32x4 tc[4], ts[4];
    if (f <= 80) {
#pragma unroll
        for (int i = 0; i < 16; ++i) {
            const int a = i >> 3, u = (i >> 2) & 1, cc = i & 3;
            const float f1 = fmaf((float)(8 * g + e) + (float)(32 * u + 2 * cc), (float)(n + 16 * a) * (1.f / 2048.f), ph);
            tc[i >> 2][cc] = __builtin_amdgcn_cosf(f1); ts[i >> 2][cc] = __builtin_amdgcn_sinf(f1);
        }
#pragma unroll
        for (int i = 0; i < 4; ++i) { PIN2(tc[i]); PIN2(ts[i]); }
    }
    int ib = 17 * GSH + (f - 81) * 8 + wid;
    u32x4 bl = {BLANK, BLANK, BLANK, BLANK};
    USE(bl);
    if (f < 64 && wid >= 5) { if (wid == 5) WAIT_VM(1); else WAIT_VM(2); }
    else WAIT_VM(0);
    BAR();
    if (wid == 0 && l == 0) set_flag(ws, FL_F1 + f, tag);
    if (f < 64) {
        if (wid != 5) { for (int i = wid - (wid > 5); i < 50; i += 7) glds16((const char*)&g_I + i * 1024 + l * 16, i * 1024); }
    } else {
        if (f > 80) {
            for (; ib < 2560; ib += 120) st128(bl, rs, l * 16, WS_GT + ib * 1024);
            WAIT_VM(0);
            BAR();
            if (wid == 0 && l == 0) set_flag(ws, FL_G + f - 64, tag);
            return;
        }
        if (wid != 5) { for (int i = wid - (wid > 5); i < GSH; i += 7) st128(bl, rs, l * 16, WS_GT + ((f - 64) * GSH + i) * 1024); }
    }
    const char* tp = lds + L_T + l * 16;
    bf16x8 T1[2][3], T2[2][3], T3[2][3], T4[8];
#pragma unroll
    for (int i = 0; i < 6; ++i) {
        T1[i / 3][i % 3] = ldsv(tp + i * 1024); T2[i / 3][i % 3] = ldsv(tp + (6 + 6 * e + i) * 1024); T3[i / 3][i % 3] = ldsv(tp + (6 + 12 * e + i) * 1024);
        USE(T1[i / 3][i % 3]); USE(T2[i / 3][i % 3]); USE(T3[i / 3][i % 3]);
    }
#pragma unroll
    for (int i = 0; i < 8; ++i) { T4[i] = ldsv(tp + (24 + i) * 1024); USE(T4[i]); }
    if (wid == 5) {
        const bool wok = wait_flags(ws, FL_F1 + l, FL_F1 + 64 + (l & 31), false, tag);
        *(LAS u32*)(lds + L_X1 + 65536) = wok;
        WAIT_LGKM0();
    } else if (f >= 64) WAIT_VM(0);
    BAR();
    if (f >= 64 && wid == 0 && l == 0) set_flag(ws, FL_G + f - 64, tag);
    const bool realonly = f == 0 || f == 80;
    const int kslot = f == 80 ? 0 : f, p0 = f == 80 ? 64 : 0;
    {
        bf16x8 xr[2], xi[2];
        const int xs = WS_XF + (kslot * 4 + c) * 8192 + e * 4096;
        u32x4 xa[2], xb[2];
        const int xo = (g & 1) * 512 + (n >> 2) * 128 + (g >> 1) * 64 + (n & 3) * 16;
#pragma unroll
        for (int u = 0; u < 2; ++u) { xa[u] = ld128(rs, xo + u * 2048, xs); xb[u] = ld128(rs, xo + u * 2048 + 1024, xs); }
        const bool wok = __builtin_amdgcn_readfirstlane(*(const LAS u32*)(lds + L_X1 + 65536));
        SCHED();
        f32x4 uc[4], us[4];
#pragma unroll
        for (int i = 0; i < 16; ++i) {
            const int a = i >> 3, u = (i >> 2) & 1, cc = i & 3;
            const float f2 = fmaf((float)(2 * n + e + 32 * u) * (1.f / 2048.f), (float)(4 * g) + (float)(16 * a + cc), ph);
            uc[i >> 2][cc] = __builtin_amdgcn_cosf(f2); us[i >> 2][cc] = __builtin_amdgcn_sinf(f2);
        }
#pragma unroll
        for (int i = 0; i < 4; ++i) { PIN2(uc[i]); PIN2(us[i]); }
        SCHED();
        if (!wok) wait_flags(ws, FL_F1 + 64 + (l & 31), tag);
        u32x2 wv[2][4];
#pragma unroll
        for (int i = 0; i < 8; ++i) wv[i & 1][i >> 1] = ld64(rs, l * 8 + (i & 1) * 512, WS_WT + ((i >> 1) * 81 + f) * 8192 + wid * 1024);
        SCHED();
#pragma unroll
        for (int u = 0; u < 2; ++u) {
#pragma unroll
            for (int k = 0; k < 4; ++k) { const auto sw = __builtin_amdgcn_permlane32_swap(xa[u][k], xb[u][k], false, false); xa[u][k] = sw[0]; xb[u][k] = sw[1]; }
            xr[u] = asfrag(f == 80 ? xb[u] : xa[u]); xi[u] = asfrag(realonly ? u32x4{0u, 0u, 0u, 0u} : xb[u]);
        }
        SCHED();
#pragma unroll
        for (int a = 0; a < 2; ++a) {
            u32x4 pr, pi;
#pragma unroll
            for (int u = 0; u < 2; ++u) {
                const f32x4 yr = MFMA16(xi[u], T1[a][1], MFMA16(xr[u], T1[a][0], z4)), yi = MFMA16(xr[u], T1[a][2], MFMA16(xi[u], T1[a][0], z4));
                const int t = 2 * a + u;
                pr[2 * u] = pk2(fma2(lo2(yr), lo2(tc[t]), lo2(yi) * lo2(ts[t]))); pr[2 * u + 1] = pk2(fma2(hi2(yr), hi2(tc[t]), hi2(yi) * hi2(ts[t])));
                pi[2 * u] = pk2(fma2(lo2(yi), lo2(tc[t]), -lo2(yr) * lo2(ts[t]))); pi[2 * u + 1] = pk2(fma2(hi2(yi), hi2(tc[t]), -hi2(yr) * hi2(ts[t])));
            }
#pragma unroll
            for (int v = 0; v < 2; ++v) {
                char* o = lds + L_X1 + (4 * v + a) * 8192 + c * 2048 + e * 1024 + l * 8;
                const f32x4 zr = MFMA16(T2[v][1], asfrag(pi), MFMA16(T2[v][0], asfrag(pr), z4)), zi = MFMA16(T2[v][2], asfrag(pr), MFMA16(T2[v][0], asfrag(pi), z4));
                *(LAS f32x2*)(o) = lo2(zr); *(LAS f32x2*)(o + 16384) = hi2(zr);
                *(LAS f32x2*)(o + 512) = lo2(zi); *(LAS f32x2*)(o + 16896) = hi2(zi);
            }
        }
        f32x2 wr[2][4], wi[2][4];
#pragma unroll
        for (int hi = 0; hi < 2; ++hi)
#pragma unroll
            for (int k = 0; k < 4; ++k) {
                wr[hi][k] = f32x2{bflo(wv[hi][k][0]), bflo(wv[hi][k][1])}; wi[hi][k] = f32x2{bfhi(wv[hi][k][0]), bfhi(wv[hi][k][1])};
                PIN2(wr[hi][k]); PIN2(wi[hi][k]);
            }
        WAIT_LGKM0();
        BAR();
        u64 gf = 0;
        if (wid == 7) gf = __hip_atomic_load((GAS u64*)(ws + WS_FL + (FL_G + (l & 31)) * FSTR), RLX_AGENT);
        {
            const char* in = lds + L_X1 + wid * 8192 + l * 8;
            f32x2 re[2][4], im[2][4];
#pragma unroll
            for (int k = 0; k < 4; ++k) {
                const f32x2 er = *(const LAS f32x2*)(in + k * 2048), ei = *(const LAS f32x2*)(in + k * 2048 + 512);
                const f32x2 dr = *(const LAS f32x2*)(in + k * 2048 + 1024), di = *(const LAS f32x2*)(in + k * 2048 + 1536);
                re[0][k] = er + dr; im[0][k] = ei + di; re[1][k] = er - dr; im[1][k] = ei - di;
            }
#pragma unroll
            for (int hi = 0; hi < 2; ++hi) {
                const f32x2 *R = re[hi], *I = im[hi], *P = wr[hi], *Q = wi[hi];
                const f32x2 ar = R[0] + R[2], ai = I[0] + I[2], br = R[0] - R[2], bi = I[0] - I[2];
                const f32x2 cr = R[1] + R[3], ci = I[1] + I[3], dr = R[1] - R[3], di = I[1] - I[3];
                const f32x2 x0r = ar + cr, x0i = ai + ci, x2r = ar - cr, x2i = ai - ci, x1r = br + di, x1i = bi - dr, x3r = br - di, x3i = bi + dr;
                const f32x2 y0r = fma2(P[0], x0r, -Q[0] * x0i), y0i = fma2(P[0], x0i, Q[0] * x0r);
                const f32x2 y1r = fma2(P[1], x1r, -Q[1] * x1i), y1i = fma2(P[1], x1i, Q[1] * x1r);
                const f32x2 sr = fma2(P[2], x2r, fma2(Q[2], x2i, y0r)), si = fma2(P[2], x2i, fma2(-Q[2], x2r, y0i));
                const f32x2 ur = fma2(P[3], x3r, fma2(Q[3], x3i, y1r)), ui = fma2(P[3], x3i, fma2(-Q[3], x3r, y1i));
                const f32x2 tr = fma2(sp(2.f), y0r, -sr), ti = fma2(sp(2.f), y0i, -si), vr = fma2(sp(2.f), y1r, -ur), vi = fma2(sp(2.f), y1i, -ui);
                re[hi][0] = sr + ur; im[hi][0] = si + ui; re[hi][2] = sr - ur; im[hi][2] = si - ui;
                re[hi][1] = tr - vi; im[hi][1] = ti + vr; re[hi][3] = tr + vi; im[hi][3] = ti - vr;
            }
#pragma unroll
            for (int o = 0; o < 4; ++o)
            {
                char* po = lds + L_T + wid * 4096 + o * 1024 + l * 8;
                *(LAS u32x2*)(po) = u32x2{pk2(re[0][o] + re[1][o]), pk2(im[0][o] + im[1][o])};
                *(LAS u32x2*)(po + 512) = u32x2{pk2(re[0][o] - re[1][o]), pk2(im[0][o] - im[1][o])};
            }
        }
        WAIT_LGKM0();
        if (wid == 7 && !__all(gf == flagval(tag))) wait_flags(ws, FL_G + (l & 31), tag);
        BAR();
        bf16x8 vf[2][2];
#pragma unroll
        for (int a = 0; a < 2; ++a) {
            u32x2 qv[4];
#pragma unroll
            for (int r = 0; r < 4; ++r) qv[r] = *(const LAS u32x2*)(lds + L_T + (2 * r + a) * 4096 + c * 1024 + e * 512 + l * 8);
            const bf16x8 ar = asfrag(qv[0][0], qv[1][0], qv[2][0], qv[3][0]), ai = asfrag(qv[0][1], qv[1][1], qv[2][1], qv[3][1]);
#pragma unroll
            for (int u = 0; u < 2; ++u) {
                const f32x4 yr = MFMA16(ai, T3[u][2], MFMA16(ar, T3[u][0], z4)), yi = MFMA16(ar, T3[u][1], MFMA16(ai, T3[u][0], z4));
                const int t = 2 * a + u;
                vf[a][u] = asfrag(pk2(fma2(lo2(yr), lo2(uc[t]), -lo2(yi) * lo2(us[t]))), pk2(fma2(hi2(yr), hi2(uc[t]), -hi2(yi) * hi2(us[t]))),
                                  pk2(fma2(lo2(yi), lo2(uc[t]), lo2(yr) * lo2(us[t]))), pk2(fma2(hi2(yi), hi2(uc[t]), hi2(yr) * hi2(us[t]))));
            }
        }
        const int gs = WS_GT + e * 40960 + kslot * 512 + c * 128;
#pragma unroll
        for (int w = 0; w < 2; ++w) {
            u32x4 v[2];
#pragma unroll
            for (int part = 0; part < 2; ++part) {
                const f32x4 o0 = MFMA16(vf[1][0], T4[4 * w + 2 + part], MFMA16(vf[0][0], T4[4 * w + part], z4)), o1 = MFMA16(vf[1][1], T4[4 * w + 2 + part], MFMA16(vf[0][1], T4[4 * w + part], z4));
                v[part] = u32x4{pk2(lo2(o0)), pk2(hi2(o0)), pk2(lo2(o1)), pk2(hi2(o1))};
            }
            if (realonly) st128(v[0], rs, n * 81920 + g * 16 + p0, gs + w * 16 * 81920);
            else {
                pair8(v[0], v[1]);
                st128(v[0], rs, (n & 7) * 81920 + (n >> 3) * 64 + g * 16, gs + w * 16 * 81920);
                st128(v[1], rs, (n & 7) * 81920 + (n >> 3) * 64 + g * 16, gs + (w * 16 + 8) * 81920);
            }
        }
    }
    if (f >= 64) { return; }
    const int mp = wid & 3, three = wid < 4, pair0 = three ? 0 : 3;
    bf16x8 bt[5][6];
#pragma unroll
    for (int ks = 0; ks < 5; ++ks)
#pragma unroll
        for (int k = 0; k < 6; ++k)
            if (k < 4 || three) { bt[ks][k] = ldsv(lds + pair0 * 2048 + l * 16 + ks * 10240 + k * 1024); USE(bt[ks][k]); }
    WAIT_VM(0);
    u32 gg[4];
    auto ldg = [&]() {
#pragma unroll
        for (int i = 0; i < 4; ++i) gg[i] = ld32(rs, (2 * (16 * (i >> 1) + 4 * mp + 2 * (i & 1) + (g & 1))) * 4, WS_OG + 8192 + (64 * (f >> 1) + (f & 1)) * 4);
    };
    {
        u32x4 gl[5];
        const int fs = FL_S1 + (f >> 2) * 10 + ((f >> 1) & 1) * 4 + (l & 3);
        const u64 sf = __hip_atomic_load((GAS u64*)(ws + WS_FL + fs * FSTR), RLX_AGENT);
#pragma unroll
        for (int it = 0; it < 5; ++it) gl[it] = ld128(rs, (wid * 64 + l) * 16, WS_GT + f * 40960 + it * 8192);
        for (int spin = 0; spin < SPIN_MAX; ++spin) {
            bool bk[5], any = false;
#pragma unroll
            for (int it = 0; it < 5; ++it) { bk[it] = blank(maxw(0u, gl[it])); any |= bk[it]; }
            if (!any) break;
            asm volatile("" ::: "memory");
#pragma unroll
            for (int it = 0; it < 5; ++it)
                if (bk[it]) gl[it] = ld128(rs, (wid * 64 + l) * 16, WS_GT + f * 40960 + it * 8192);
        }
        if (!__all(sf == flagval(tag))) wait_flags(ws, fs, tag);
        __builtin_amdgcn_fence(__ATOMIC_ACQUIRE, "wavefront");
        ldg();
        const int L = wid * 64 + l;
#pragma unroll
        for (int it = 0; it < 5; ++it) *(LAS u32x4*)(lds + L_X1 + it * 8192 + ((L ^ ((L >> 5) & 3)) * 16)) = gl[it];
    }
    WAIT_LGKM0();
    BAR();
    {
        f32x4 acc[2][6];
        const int r = n >> 2, kq = (2 * g + (r >> 1)) & 3;
        const char* ap = lds + L_X1 + (2 * g + (r >> 1)) * 512 + (n & 3) * 128 + (r & 1) * 64 + ((mp ^ kq) * 16);
        bf16x8 a0[5], a1[5];
#pragma unroll
        for (int ks = 0; ks < 5; ++ks) { a0[ks] = trfrag(ap + ks * 8192, ap + ks * 8192 + 4096); a1[ks] = trfrag(ap + ks * 8192 + 8, ap + ks * 8192 + 4104); }
#pragma unroll
        for (int pp = 0; pp < 3; ++pp)
            if (pp < 2 || three) {
#pragma unroll
                for (int ks = 0; ks < 5; ++ks)
#pragma unroll
                    for (int k = 2 * pp; k < 2 * pp + 2; ++k) { acc[0][k] = MFMA16(a0[ks], bt[ks][k], ks ? acc[0][k] : z4); acc[1][k] = MFMA16(a1[ks], bt[ks][k], ks ? acc[1][k] : z4); }
#pragma unroll
                for (int mt = 0; mt < 2; ++mt) {
                    const int tok = 64 * (f >> 1) + 2 * (16 * mt + 4 * mp + g) + (f & 1);
                    u32x4 v;
#pragma unroll
                    for (int h = 0; h < 2; ++h) {
                        const f32x4 x4 = acc[mt][2 * pp + h];
                        const auto s0 = __builtin_amdgcn_permlane16_swap(f2u(x4[0]), f2u(x4[1]), false, false);
                        const auto s1 = __builtin_amdgcn_permlane16_swap(f2u(x4[2]), f2u(x4[3]), false, false);
                        const u32 q0 = pk2(sp(u2f(gg[2 * mt])) * f32x2{u2f(s0[0]), u2f(s0[1])}), q1 = pk2(sp(u2f(gg[2 * mt + 1])) * f32x2{u2f(s1[0]), u2f(s1[1])});
                        const auto s2 = __builtin_amdgcn_permlane32_swap(q0, q1, false, false);
                        v[2 * h] = s2[0]; v[2 * h + 1] = s2[1];
                    }
                    st128(v, rs, tok * 1280 + n * 16 + pp * 256, WS_R + pair0 * 256);
                }
            }
    }
    WAIT_VM(0);
    BAR();
    if (wid == 0 && l == 0) set_flag(ws, FL_F3 + f, tag);
}

__global__ __launch_bounds__(512, 2) void fused(Args A0) {
    extern __shared__ __attribute__((aligned(16))) char lds[];
    const Args& A = A0;
    const int l = threadIdx.x & 63, wid = __builtin_amdgcn_readfirstlane(threadIdx.x >> 6);
    const int xcd = blockIdx.x & 7, j = blockIdx.x >> 3;
    char* ws = A.ws;
    if (wid == 3 && j < 6) touch((const void*)__builtin_amdgcn_s_getpc(), min((j * 64 + l) * 128, 42 * 1024));
    asm volatile("" ::"s"(A.x), "s"(A.dsk), "s"(A.ws));
    if (j < 20) s_chain(A, lds, xcd, j, wid, l);
    else f_chain(A, lds, (j - 20) * 8 + xcd, wid, l);
}

extern "C" void kernel_launch(void* const* d_in, const int* in_sizes, int n_in, void* d_out, int out_size, void* d_ws, size_t ws_size,
                              hipStream_t stream) {
    Args A;
    A.x = (const u16*)d_in[0]; A.wr = (const u16*)d_in[1]; A.wi = (const u16*)d_in[2];
    A.lre = (const u16*)d_in[4]; A.lim = (const u16*)d_in[5]; A.bre = (const u16*)d_in[6]; A.bim = (const u16*)d_in[7];
    A.cre = (const u16*)d_in[8]; A.cim = (const u16*)d_in[9]; A.ldt = (const u16*)d_in[10]; A.dsk = (const u16*)d_in[11]; A.gw = (const u16*)d_in[12];
    A.ws = (char*)d_ws; A.out = (float*)d_out;
    (void)hipFuncSetAttribute((const void*)fused, hipFuncAttributeMaxDynamicSharedMemorySize, 163840);
    fused<<<256, 512, 163840, stream>>>(A);
}
```
